```python
import torch

def _softmax(x, axis=-1):
    return torch.softmax(x, dim=axis)
def _gelu(x):
    return torch.nn.functional.gelu(x, approximate="tanh")
def _silu(x):
    return torch.nn.functional.silu(x)
def _sigmoid(x):
    return torch.sigmoid(x)
def _layernorm(x, axis=-1, eps=1e-5):
    m = x.mean(dim=axis, keepdim=True)
    v = x.var(dim=axis, keepdim=True, unbiased=False)
    return (x - m) / torch.sqrt(v + eps)
def _scatter_add(tgt, idx, src, axis=0):
    return tgt.clone().index_add_(axis, idx, src)
def _segment_sum(src, seg, n):
    out = torch.zeros((n,) + src.shape[1:], dtype=src.dtype)
    return out.index_add_(0, seg, src)
def _pad(x, pw):
    flat = tuple(v for pair in reversed(pw) for v in pair)
    return torch.nn.functional.pad(x, flat)

kernel_name = 'sequential_preLN__sliding_block_cached__glu_gated_glu__decode'

def setup_inputs():

    _w = (lambda a: (lambda t: t.to(torch.bfloat16) if t.is_floating_point() else t)(torch.as_tensor(a)))
    def _A_setup():
        B, Sq, D = 8, 1, 1024
        Hq, Hkv, Sk, Dh = 32, 4, 4096, 128
        assert Hq % Hkv == 0, (Hq, Hkv)
        return {
            "x": _w((lambda *s: torch.randn(*s, dtype=torch.bfloat16) * 0.05)(B, Sq, D)),
            "k_cache": _w((lambda *s: torch.randn(*s, dtype=torch.bfloat16) * 3.2)(B, Hkv, Sk, Dh)),
            "v_cache": _w((lambda *s: torch.randn(*s, dtype=torch.bfloat16) * 0.4)(B, Hkv, Sk, Dh)),
            "wq": _w((lambda *s: torch.randn(*s, dtype=torch.bfloat16) * 3.2)(D, Hq * Dh) * (D ** -0.5)),
            "wo": _w((lambda *s: torch.randn(*s, dtype=torch.bfloat16) * 0.4)(Hq * Dh, D) * ((Hq * Dh) ** -0.5)),
        }
    _A_in = _A_setup()

    def _B_setup():
        B, S, D, F = 8, 1, 1024, 3584
        return {
            "x": _w((lambda *s: torch.randn(*s, dtype=torch.bfloat16) * 0.05)(B, S, D)),
            "w_gate": _w((lambda *s: torch.randn(*s, dtype=torch.bfloat16) * 0.05)(D, F)),
            "w_up": _w((lambda *s: torch.randn(*s, dtype=torch.bfloat16) * 0.05)(D, F)),
            "w_down": _w((lambda *s: torch.randn(*s, dtype=torch.bfloat16) * 0.05)(F, D)),
        }
    _B_in = _B_setup()

    return {
        "A_x": _A_in["x"],
        "A_k_cache": _A_in["k_cache"],
        "A_v_cache": _A_in["v_cache"],
        "A_wq": _A_in["wq"],
        "A_wo": _A_in["wo"],
        "B_x": _B_in["x"],
        "B_w_gate": _B_in["w_gate"],
        "B_w_up": _B_in["w_up"],
        "B_w_down": _B_in["w_down"],
    }

def reference(A_x, A_k_cache, A_v_cache, A_wq, A_wo, B_x, B_w_gate, B_w_up, B_w_down):
    def _A_ref(x, k_cache, v_cache, wq, wo):
        B, Sq, D = x.shape
        Hq, Dh = 32, 128
        Hkv, Sk = k_cache.shape[1], k_cache.shape[2]
        W = min(4096, max(256, Sk // 4))
        rep = Hq // Hkv
        q = (lambda x, *d: x.permute(*d))((x @ wq).reshape(B, Sq, Hq, Dh), 0, 2, 1, 3)
        k = (lambda x, n, axis: torch.repeat_interleave(x, n, dim=axis))(k_cache[:, :, Sk - W:, :], rep, axis=1)
        v = (lambda x, n, axis: torch.repeat_interleave(x, n, dim=axis))(v_cache[:, :, Sk - W:, :], rep, axis=1)
        scores = torch.einsum("bhqd,bhkd->bhqk", q, k) * (Dh ** -0.5)
        p = _softmax(scores, axis=-1)
        o = torch.einsum("bhqk,bhkd->bhqd", p, v)
        return (lambda x, *d: x.permute(*d))(o, 0, 2, 1, 3).reshape(B, Sq, Hq * Dh) @ wo

    def _B_ref(x, w_gate, w_up, w_down):
        g = torch.einsum("bsd,df->bsf", x, w_gate)
        u = torch.einsum("bsd,df->bsf", x, w_up)
        h = _sigmoid(g) * u
        return torch.einsum("bsf,fd->bsd", h, w_down)

    x = A_x
    h = x + _A_ref(_layernorm(x, axis=-1), A_k_cache, A_v_cache, A_wq, A_wo)
    h2 = h + _B_ref(_layernorm(h, axis=-1), B_w_gate, B_w_up, B_w_down)
    return h2
```

```cpp
#include <hip/hip_runtime.h>
#include <hip/hip_bf16.h>
#include <stdint.h>

#define MIMIC 0

typedef unsigned short u16;
__device__ __forceinline__ float bf2f(u16 v) { return __builtin_bit_cast(float, (unsigned)v << 16); }
__device__ __forceinline__ u16 f2bf(float f) { __bf16 b = (__bf16)f; return __builtin_bit_cast(u16, b); }
__device__ __forceinline__ float rb(float f) {
  return f;
}

constexpr int B = 8, D = 1024, HQ = 32, HKV = 4, SK = 4096, DH = 128, W = 1024, F = 3584;

__device__ float block_sum(float v, float* red) {
  for (int o = 32; o > 0; o >>= 1) v += __shfl_xor(v, o);
  int w = threadIdx.x >> 6;
  __syncthreads();
  if ((threadIdx.x & 63) == 0) red[w] = v;
  __syncthreads();
  float s = 0;
  for (int i = 0; i < (int)(blockDim.x >> 6); ++i) s += red[i];
  return s;
}
__device__ float block_max(float v, float* red) {
  for (int o = 32; o > 0; o >>= 1) v = fmaxf(v, __shfl_xor(v, o));
  int w = threadIdx.x >> 6;
  __syncthreads();
  if ((threadIdx.x & 63) == 0) red[w] = v;
  __syncthreads();
  float s = -INFINITY;
  for (int i = 0; i < (int)(blockDim.x >> 6); ++i) s = fmaxf(s, red[i]);
  return s;
}

__global__ void k_ln(const float* in, float* out) {
  __shared__ float red[16];
  int b = blockIdx.x;
  float v[4];
  float s = 0;
  for (int i = 0; i < 4; ++i) { v[i] = in[b * D + threadIdx.x + 256 * i]; s += v[i]; }
  float mean = block_sum(s, red) / D;
  float q = 0;
  for (int i = 0; i < 4; ++i) { float d = v[i] - mean; q += d * d; }
  float var = block_sum(q, red) / D;
  float m = rb(mean), vv = rb(var);
  float den = rb(sqrtf(rb(vv + 1e-5f)));
  for (int i = 0; i < 4; ++i) out[b * D + threadIdx.x + 256 * i] = rb(rb(v[i] - m) / den);
}
__global__ void k_cvt(const u16* in, float* out, int n) {
  int i = blockIdx.x * 256 + threadIdx.x;
  if (i < n) out[i] = bf2f(in[i]);
}
__global__ void k_gemv(const float* a, const u16* w, float* out, int K, int N) {
  __shared__ float red[4][8][64];
  int n = blockIdx.x * 64 + (threadIdx.x & 63);
  int ks = threadIdx.x >> 6;
  float acc[8] = {0};
  for (int k = ks; k < K; k += 4) {
    float wv = bf2f(w[(size_t)k * N + n]);
    for (int b = 0; b < 8; ++b) acc[b] += a[b * K + k] * wv;
  }
  for (int b = 0; b < 8; ++b) red[ks][b][threadIdx.x & 63] = acc[b];
  __syncthreads();
  if (ks == 0) for (int b = 0; b < 8; ++b) {
    int l = threadIdx.x & 63;
    out[b * N + n] = rb(red[0][b][l] + red[1][b][l] + red[2][b][l] + red[3][b][l]);
  }
}
__global__ void k_attn(const float* q, const u16* kc, const u16* vc, float* o) {
  __shared__ float red[16];
  __shared__ float p[W];
  __shared__ float qs[DH];
  int h = blockIdx.x, b = blockIdx.y, g = h / (HQ / HKV);
  const u16* K = kc + ((size_t)(b * HKV + g) * SK + (SK - W)) * DH;
  const u16* V = vc + ((size_t)(b * HKV + g) * SK + (SK - W)) * DH;
  if (threadIdx.x < DH) qs[threadIdx.x] = q[b * HQ * DH + h * DH + threadIdx.x];
  __syncthreads();
  float sc[4]; float mx = -INFINITY;
  const float scale = 0.08838834764831845f;
  for (int i = 0; i < 4; ++i) {
    int key = threadIdx.x + 256 * i;
    float s = 0;
    for (int d = 0; d < DH; ++d) s += qs[d] * bf2f(K[key * DH + d]);
    s = rb(rb(s) * scale);
    sc[i] = s; mx = fmaxf(mx, s);
  }
  mx = block_max(mx, red);
  float sum = 0;
  for (int i = 0; i < 4; ++i) { sc[i] = expf(sc[i] - mx); sum += sc[i]; }
  sum = block_sum(sum, red);
  for (int i = 0; i < 4; ++i) p[threadIdx.x + 256 * i] = rb(sc[i] / sum);
  __syncthreads();
  if (threadIdx.x < DH) {
    float acc = 0;
    for (int key = 0; key < W; ++key) acc += p[key] * bf2f(V[key * DH + threadIdx.x]);
    o[b * HQ * DH + h * DH + threadIdx.x] = rb(acc);
  }
}
__global__ void k_add(const float* a, const float* b, float* c, int n) {
  int i = blockIdx.x * 256 + threadIdx.x;
  if (i < n) c[i] = rb(a[i] + b[i]);
}
__global__ void k_glu(const float* g, const float* u, float* c, int n) {
  int i = blockIdx.x * 256 + threadIdx.x;
  if (i < n) c[i] = rb(rb(1.f / (1.f + expf(-g[i]))) * u[i]);
}
__global__ void k_out(const float* a, const float* b, u16* c, int n) {
  int i = blockIdx.x * 256 + threadIdx.x;
  if (i < n) c[i] = f2bf(a[i] + b[i]);
}

extern "C" void kernel_launch(void* const* d_in, const int* in_sizes, int n_in,
                              void* d_out, int out_size, void* d_ws, size_t ws_size,
                              hipStream_t stream) {
  const u16* x = (const u16*)d_in[0];
  const u16* kc = (const u16*)d_in[1];
  const u16* vc = (const u16*)d_in[2];
  const u16* wq = (const u16*)d_in[3];
  const u16* wo = (const u16*)d_in[4];
  const u16* wg = (const u16*)d_in[6];
  const u16* wu = (const u16*)d_in[7];
  const u16* wd = (const u16*)d_in[8];
  float* ws = (float*)d_ws;
  float* xf = ws; float* xn = xf + 8192; float* q = xn + 8192; float* o = q + 32768;
  float* ao = o + 32768; float* h = ao + 8192; float* hn = h + 8192; float* g = hn + 8192;
  float* u = g + 8 * F; float* hh = u + 8 * F; float* dn = hh + 8 * F;
  k_cvt<<<32, 256, 0, stream>>>(x, xf, 8192);
  k_ln<<<8, 256, 0, stream>>>(xf, xn);
  k_gemv<<<4096 / 64, 256, 0, stream>>>(xn, wq, q, 1024, 4096);
  k_attn<<<dim3(32, 8), 256, 0, stream>>>(q, kc, vc, o);
  k_gemv<<<1024 / 64, 256, 0, stream>>>(o, wo, ao, 4096, 1024);
  k_add<<<32, 256, 0, stream>>>(xf, ao, h, 8192);
  k_ln<<<8, 256, 0, stream>>>(h, hn);
  k_gemv<<<F / 64, 256, 0, stream>>>(hn, wg, g, 1024, F);
  k_gemv<<<F / 64, 256, 0, stream>>>(hn, wu, u, 1024, F);
  k_glu<<<8 * F / 256, 256, 0, stream>>>(g, u, hh, 8 * F);
  k_gemv<<<1024 / 64, 256, 0, stream>>>(hh, wd, dn, F, 1024);
  k_out<<<32, 256, 0, stream>>>(h, dn, (u16*)d_out, 8192);
}
```

```cpp
#include <hip/hip_runtime.h>
#include <stdint.h>

#define GAS __attribute__((address_space(1)))
#define LAS __attribute__((address_space(3)))
typedef unsigned v4u __attribute__((ext_vector_type(4)));
typedef unsigned v2u __attribute__((ext_vector_type(2)));
typedef float f32x4 __attribute__((ext_vector_type(4)));
typedef float f32x2 __attribute__((ext_vector_type(2)));
typedef __bf16 bf16x8 __attribute__((ext_vector_type(8)));
typedef __bf16 bf16x2 __attribute__((ext_vector_type(2)));
typedef unsigned long long u64;
typedef unsigned char u8;

#define W_NT 1
#define K_NT 1
#define DEPTH 3
#define CODE_LINES 120
#define PSLEEP 2

constexpr unsigned WS_EPOCH = 0, WS_F1 = 4096, WS_F2 = 8192, ARENA = 65536;
constexpr unsigned QP = ARENA;
constexpr unsigned OP = QP + 512 * 1024;
constexpr unsigned WP = OP + 512 * 1024;
constexpr unsigned DP = WP + 1024 * 1024;
constexpr unsigned GP = DP + 896 * 1024;
constexpr unsigned ML = GP + 224 * 1024;
constexpr unsigned HF = ML + 16 * 1024;
constexpr unsigned POISON_KB = 11;
constexpr unsigned WS_END = OP + 256 * POISON_KB * 1024;
static_assert(HF + 16 * 1024 <= WS_END, "arena");
constexpr unsigned WS_STAMP = 16u << 20;

constexpr int L_CTL = 0, L_XN = 256, L_QF = L_XN + 4096, L_STA = L_QF + 2048, L_STG = L_STA + 256;
constexpr int L_RQ = L_STG + 2048, L_RA = L_RQ + 8192, L_RO = L_RA + 16384, L_RD = L_RO + 16384, L_RG = L_RD + 16384;
constexpr int L_USED = L_RG + 32768, L_END = 100 * 1024;
static_assert(L_USED <= L_END, "lds");
enum { F_TOK = 0, F_PZ = 32, F_GO1 = 40, F_GO2 = 48, F_XN = 56, F_Q = 64, F_QPUB = 72, F_QF = 80, F_S = 88, F_A = 96, F_APUB = 104,
       F_O = 112, F_HPUB = 120, F_G0 = 128, F_G1 = 136, F_GPUB = 144, F_D = 152, F_DPUB = 160, F_TAG = 168 };

constexpr float LOG2E = 1.4426950408889634f;
constexpr unsigned SPIN_G = 1u << 16, SPIN_L = 1u << 22;

__device__ __forceinline__ float asf(unsigned u) { return __builtin_bit_cast(float, u); }
__device__ __forceinline__ unsigned asu(float f) { return __builtin_bit_cast(unsigned, f); }
__device__ __forceinline__ float lo16(unsigned u) { return asf(u << 16); }
__device__ __forceinline__ float hi16(unsigned u) { return asf(u & 0xffff0000u); }
__device__ __forceinline__ unsigned pk(float a, float b) { bf16x2 v = {(__bf16)a, (__bf16)b}; return __builtin_bit_cast(unsigned, v); }
__device__ __forceinline__ float rb(float a) { return (float)(__bf16)a; }
__device__ __forceinline__ unsigned nz(unsigned u) { return u < 0xfffffffeu ? u : 0xfffffffeu; }
template <int CTRL> __device__ __forceinline__ float dpp(float x) {
  return __builtin_bit_cast(float, __builtin_amdgcn_mov_dpp(__builtin_bit_cast(int, x), CTRL, 0xf, 0xf, true));
}
template <int BANK> __device__ __forceinline__ unsigned ror8z(unsigned x) {
  return (unsigned)__builtin_amdgcn_update_dpp(0, (int)x, 0x128, 0xf, BANK, false);
}
__device__ __forceinline__ float x32_sum(float x) { auto t = __builtin_amdgcn_permlane32_swap(asu(x), asu(x), false, false); return asf(t[0]) + asf(t[1]); }
__device__ __forceinline__ float x16_sum(float x) { auto t = __builtin_amdgcn_permlane16_swap(asu(x), asu(x), false, false); return asf(t[0]) + asf(t[1]); }
__device__ __forceinline__ float x32_max(float x) { auto t = __builtin_amdgcn_permlane32_swap(asu(x), asu(x), false, false); return fmaxf(asf(t[0]), asf(t[1])); }
__device__ __forceinline__ float x16_max(float x) { auto t = __builtin_amdgcn_permlane16_swap(asu(x), asu(x), false, false); return fmaxf(asf(t[0]), asf(t[1])); }
__device__ __forceinline__ float wave_sum(float x) {
  x += dpp<0xB1>(x); x += dpp<0x4E>(x); x += dpp<0x141>(x); x += dpp<0x128>(x);
  return x32_sum(x16_sum(x));
}
__device__ __forceinline__ f32x4 mfma(v4u a, v4u b, f32x4 c) {
  return __builtin_amdgcn_mfma_f32_16x16x32_bf16(__builtin_bit_cast(bf16x8, a), __builtin_bit_cast(bf16x8, b), c, 0, 0, 0);
}
#define CBAR() asm volatile("" ::: "memory")
#define VM0() asm volatile("s_waitcnt vmcnt(0)" ::: "memory")
#define SB() __builtin_amdgcn_sched_barrier(0)
#define PRIO(n) __builtin_amdgcn_s_setprio(n)
#define TILE(p, bytes) __builtin_amdgcn_make_buffer_rsrc((void*)(p), 0, (int)(bytes), 0x00020000)
#define LDW4(rw, lo, so, NT) __builtin_amdgcn_raw_buffer_load_b128(rw, (int)(lo), (int)(so), (NT) ? 2 : 0)
#define LDW2(rw, lo, so, NT) __builtin_amdgcn_raw_buffer_load_b64(rw, (int)(lo), (int)(so), (NT) ? 2 : 0)

__device__ __forceinline__ void tr8(const v4u (&R)[8], v4u (&F)[8]) {
#pragma unroll
  for (int d = 0; d < 4; ++d)
#pragma unroll
    for (int m = 0; m < 4; ++m) {
      F[2 * d][m] = __builtin_amdgcn_perm(R[2 * m + 1][d], R[2 * m][d], 0x05040100u);
      F[2 * d + 1][m] = __builtin_amdgcn_perm(R[2 * m + 1][d], R[2 * m][d], 0x07060302u);
    }
}
__device__ __forceinline__ void tr4(const v2u (&R)[16], v4u (&F)[8]) {
#pragma unroll
  for (int t = 0; t < 2; ++t)
#pragma unroll
    for (int d = 0; d < 2; ++d)
#pragma unroll
      for (int m = 0; m < 4; ++m) {
        F[4 * t + 2 * d][m] = __builtin_amdgcn_perm(R[8 * t + 2 * m + 1][d], R[8 * t + 2 * m][d], 0x05040100u);
        F[4 * t + 2 * d + 1][m] = __builtin_amdgcn_perm(R[8 * t + 2 * m + 1][d], R[8 * t + 2 * m][d], 0x07060302u);
      }
}
__device__ __forceinline__ void arrive(LAS u8* L, int off) {
  CBAR();
  *(volatile LAS u8*)(L + L_CTL + off) = 1;
  CBAR();
}
template <int N> __device__ __forceinline__ bool peek(LAS u8* L, int off) {
  if (N == 1) return __builtin_amdgcn_readfirstlane(*(volatile LAS u8*)(L + L_CTL + off)) != 0;
  if (N == 2) return __builtin_amdgcn_readfirstlane(*(volatile LAS unsigned short*)(L + L_CTL + off)) == 0x0101;
  if (N == 4) return __builtin_amdgcn_readfirstlane(*(volatile LAS unsigned*)(L + L_CTL + off)) == 0x01010101;
  const v2u v = *(volatile LAS v2u*)(L + L_CTL + off);
  return __builtin_amdgcn_readfirstlane(v[0] & v[1]) == 0x01010101;
}
template <int N, int SLEEP> __device__ __forceinline__ void waitf(LAS u8* L, int off) {
#pragma nounroll
  for (unsigned it = 0; it < SPIN_L; ++it) {
    if (peek<N>(L, off)) break;
    if (SLEEP) __builtin_amdgcn_s_sleep(SLEEP);
  }
  CBAR();
}
__device__ __forceinline__ unsigned mx4(unsigned m, v4u a) { return max(max(max(m, a[0]), max(a[1], a[2])), a[3]); }
__device__ __forceinline__ unsigned mx2(unsigned m, v2u a) { return max(max(m, a[0]), a[1]); }
#define LD128(off) __builtin_amdgcn_raw_buffer_load_b128(rs, (int)(off), 0, 16)
#define LD64(off) __builtin_amdgcn_raw_buffer_load_b64(rs, (int)(off), 0, 16)
#define ST128(v, off) __builtin_amdgcn_raw_buffer_store_b128(v, rs, (int)(off), 0, 16)
#define ST64(v, off) __builtin_amdgcn_raw_buffer_store_b64(v, rs, (int)(off), 0, 16)
#define POLL(...) do { bool ok_ = false; _Pragma("nounroll") for (unsigned it_ = 0; it_ < SPIN_G; ++it_) { CBAR(); \
    if (!ok_) { unsigned m_ = 0; __VA_ARGS__; ok_ = m_ != 0xffffffffu; } \
    if (__builtin_amdgcn_ballot_w64(!ok_) == 0) break; __builtin_amdgcn_s_sleep(PSLEEP); } CBAR(); } while (0)
__device__ __forceinline__ v4u pub4(f32x4 v) { return v4u{nz(asu(v[0])), nz(asu(v[1])), nz(asu(v[2])), nz(asu(v[3]))}; }
__device__ __forceinline__ f32x4 ld4(LAS u8* p) { return *(LAS f32x4*)p; }

#define TS_DECL
#define TS(i)
#define TS_OUT()
#define START_BARRIER() do { asm volatile("s_waitcnt lgkmcnt(0)" ::: "memory"); __builtin_amdgcn_s_barrier(); CBAR(); } while (0)

__global__ void __launch_bounds__(1024, 4)
fused_decode_kernel(const u8* __restrict__ wq, const u8* __restrict__ x, u8* __restrict__ ws, const u8* __restrict__ kc,
                    const u8* __restrict__ vc, const u8* __restrict__ wo, const u8* __restrict__ wg, const u8* __restrict__ wu,
                    const u8* __restrict__ wd, u8* __restrict__ out) {
  extern __shared__ __attribute__((aligned(16))) u8 smem[];
  LAS u8* L = (LAS u8*)smem;
  u64 pc0;
  asm volatile("s_getpc_b64 %0" : "=s"(pc0));
  const int tid = threadIdx.x, lane = tid & 63, w = __builtin_amdgcn_readfirstlane(tid >> 6);
  const int c = blockIdx.x, xcd = c & 7, idx = c >> 3;
  TS_DECL;
  TS(0);
  const int ksq = idx & 3, cbq = xcd * 8 + (idx >> 2);
  const __amdgpu_buffer_rsrc_t rs = __builtin_amdgcn_make_buffer_rsrc((void*)ws, 0, (int)WS_END, 0x00020000);
  const int b7 = lane & 7, h3 = (lane >> 3) & 1, kg = lane >> 4, c15 = lane & 15;
  const unsigned mlo = h3 ? 0u : 0xffffffffu, mhi = ~mlo;
  const int u224 = xcd * 28 + idx;
  const bool mlp = idx < 28;
  const f32x4 zero4 = {0.f, 0.f, 0.f, 0.f};
  const v4u empty4 = {0xffffffffu, 0xffffffffu, 0xffffffffu, 0xffffffffu};
  const int bg = xcd * 4 + (idx >> 3);

  if (__builtin_expect(w < 4, 1)) {
    v4u F[8];
    {
      v2u R[16];
      const __amdgpu_buffer_rsrc_t rw = TILE(wq + (size_t)(256 * ksq + 64 * w) * 8192 + cbq * 128, 63 * 8192 + 128);
      const unsigned lo = (unsigned)(kg * 8) * 8192u + c15 * 8;
      START_BARRIER();
#pragma unroll
      for (int j = 0; j < 16; ++j) R[j] = LDW2(rw, lo, (32 * (j >> 3) + (j & 7)) * 8192, W_NT);
      SB();
      PRIO(3);
      waitf<8, 1>(L, F_XN);
      const v4u x0 = *(LAS v4u*)(L + L_XN + w * 1024 + kg * 128 + b7 * 16), x1 = *(LAS v4u*)(L + L_XN + w * 1024 + 512 + kg * 128 + b7 * 16);
      const v4u l0 = x0 & mlo, h0 = x0 & mhi, l1 = x1 & mlo, h1 = x1 & mhi;
      waitf<1, 1>(L, F_PZ);
      VM0();
      arrive(L, F_TOK + w);
      TS(1);
      tr4(R, F);
      LAS u8* p = L + L_RQ + w * 2048 + (kg >> 1) * 1024 + b7 * 128;
#pragma unroll
      for (int i = 0; i < 2; ++i) {
        const f32x4 a = mfma(F[6 + i], h1, mfma(F[4 + i], l1, mfma(F[2 + i], h0, mfma(F[i], l0, zero4))));
        *(LAS f32x4*)(p + (((i * 4 + (kg & 1) * 2 + h3) ^ b7) * 16)) = a;
      }
    }
    arrive(L, F_Q + w);
    if (w < 2) {
      waitf<4, 0>(L, F_Q);
      LAS u8* p = L + L_RQ + w * 1024 + (lane >> 3) * 128 + ((b7 ^ (lane >> 3)) * 16);
      const f32x4 v = (ld4(p) + ld4(p + 2048)) + (ld4(p + 4096) + ld4(p + 6144));
      ST128(pub4(v), QP + ((lane >> 3) * 32 + (cbq >> 1)) * 2048 + ksq * 512 + (2 * (cbq & 1) + w) * 128 + b7 * 16);
      arrive(L, F_QPUB + w);
    }
    PRIO(0);
    TS(2);
    const int ho = bg, cbo = idx & 7;
    const int rcol = 128 * (c >> 5) + 32 * ((c >> 3) & 3) + (c & 7);
    float xr[4];
    {
      v4u R[8];
      const __amdgpu_buffer_rsrc_t rw = TILE(wo + (size_t)(128 * ho + w) * 2048 + cbo * 256, 124 * 2048 + 256);
      const unsigned lo = (unsigned)(32 * kg) * 2048u + c15 * 16;
      waitf<1, 1>(L, F_TOK + 12 + w - DEPTH);
#pragma unroll
      for (int j = 0; j < 8; ++j) R[j] = LDW4(rw, lo, (8 * (j & 3) + 4 * (j >> 2)) * 2048, W_NT);
      SB();
#pragma unroll
      for (int r = 0; r < 4; ++r) xr[r] = lo16(*(const GAS unsigned short*)(x + (b7 * 1024 + rcol + 8 * r) * 2));
      VM0();
      arrive(L, F_TOK + 12 + w);
      TS(3);
      PRIO(3);
      tr8(R, F);
    }
    waitf<1, 1>(L, F_GO2);
    float w0, w1, w2, w3;
    {
      v2u ml = {0u, 0u};
      const unsigned moff = ML + ho * 512 + (b7 * 8 + 4 * h3 + kg) * 8;
      POLL(ml = LD64(moff); m_ = mx2(m_, ml));
      const float ms = asf(ml[0]);
      const float M = x32_max(x16_max(fmaxf(ms, dpp<0x128>(ms))));
      const float wg_ = __builtin_amdgcn_exp2f((ms - M) * LOG2E) * asf(ml[1]);
      const float tot = x32_sum(x16_sum(wg_ + dpp<0x128>(wg_)));
      const float wn = wg_ * __builtin_amdgcn_rcpf(tot);
      auto t = __builtin_amdgcn_permlane32_swap(asu(wn), asu(wn), false, false);
      auto a01 = __builtin_amdgcn_permlane16_swap(t[0], t[0], false, false);
      auto a23 = __builtin_amdgcn_permlane16_swap(t[1], t[1], false, false);
      w0 = asf(a01[0]); w1 = asf(a01[1]); w2 = asf(a23[0]); w3 = asf(a23[1]);
    }
    TS(4);
    waitf<4, 1>(L, F_APUB);
    {
      v4u o[4] = {empty4, empty4, empty4, empty4};
      const unsigned ooff = OP + ((ho * 8 + b7) * 8 + 4 * h3) * 256 + w * 64 + kg * 16;
      POLL(_Pragma("unroll") for (int i = 0; i < 4; ++i) o[i] = LD128(ooff + i * 256);
           _Pragma("unroll") for (int i = 0; i < 4; ++i) m_ = mx4(m_, o[i]));
      TS(5);
      v4u bo;
#pragma unroll
      for (int d = 0; d < 4; ++d) {
        float a = w0 * lo16(o[0][d]), bq = w0 * hi16(o[0][d]);
        a = fmaf(w1, lo16(o[1][d]), a); bq = fmaf(w1, hi16(o[1][d]), bq);
        a = fmaf(w2, lo16(o[2][d]), a); bq = fmaf(w2, hi16(o[2][d]), bq);
        a = fmaf(w3, lo16(o[3][d]), a); bq = fmaf(w3, hi16(o[3][d]), bq);
        a += dpp<0x128>(a); bq += dpp<0x128>(bq);
        bo[d] = pk(a, bq);
      }
      const v4u bl = bo & mlo, bh = bo & mhi;
#pragma unroll
      for (int i = 0; i < 4; ++i) *(LAS f32x4*)(L + L_RO + w * 4096 + i * 1024 + lane * 16) = mfma(F[4 + i], bh, mfma(F[i], bl, zero4));
    }
    arrive(L, F_O + w);
    waitf<4, 0>(L, F_O);
    {
      LAS u8* p = L + L_RO + w * 1024 + lane * 16;
      const f32x4 v = (ld4(p) + ld4(p + 4096)) + (ld4(p + 8192) + ld4(p + 12288));
      ST128(pub4(v), WP + (cbo * 32 + kg * 8 + w + 4 * h3) * 4096 + ho * 128 + b7 * 16);
    }
    TS(6);
    if (w == 0) {
      v4u pr[4] = {empty4, empty4, empty4, empty4};
      POLL(_Pragma("unroll") for (int i = 0; i < 4; ++i) pr[i] = LD128(WP + c * 4096 + i * 1024 + lane * 16);
           _Pragma("unroll") for (int i = 0; i < 4; ++i) m_ = mx4(m_, pr[i]));
      TS(7);
      float h4[4];
#pragma unroll
      for (int r = 0; r < 4; ++r) {
        float s = (asf(pr[0][r]) + asf(pr[1][r])) + (asf(pr[2][r]) + asf(pr[3][r]));
        s += dpp<0x128>(s);
        s = x32_sum(x16_sum(s));
        h4[r] = rb(xr[r] + rb(s));
      }
      {
        const int g = (c >> 3) & 3, i = c & 7, t = 2 * (c >> 5) + (g >> 1);
        if (lane < 8) ST64((v2u{nz(pk(h4[0], h4[1])), nz(pk(h4[2], h4[3]))}), HF + t * 1024 + ((i >> 1) * 16 + (g & 1) * 8 + lane) * 16 + (i & 1) * 8);
      }
      arrive(L, F_HPUB);
      TS(8);
      PRIO(0);
      const int rr = (lane >> 3) & 3;
      const float hs = rr == 0 ? h4[0] : rr == 1 ? h4[1] : rr == 2 ? h4[2] : h4[3];
      if (mlp) waitf<4, 1>(L, F_DPUB);
      PRIO(3);
      TS(9);
      const unsigned off3 = lane < 32 ? DP + c * 3584 + 3072 + lane * 16 : 0xfffffff0u;
      POLL(_Pragma("unroll") for (int i = 0; i < 3; ++i) pr[i] = LD128(DP + c * 3584 + i * 1024 + lane * 16);
           pr[3] = LD128(off3);
           _Pragma("unroll") for (int i = 0; i < 4; ++i) m_ = mx4(m_, pr[i]));
      TS(10);
      float d4[4];
#pragma unroll
      for (int r = 0; r < 4; ++r) {
        float s = (asf(pr[0][r]) + asf(pr[1][r])) + (asf(pr[2][r]) + asf(pr[3][r]));
        s += dpp<0x128>(s);
        d4[r] = x32_sum(x16_sum(s));
      }
      const float ds = rr == 0 ? d4[0] : rr == 1 ? d4[1] : rr == 2 ? d4[2] : d4[3];
      const __bf16 res = (__bf16)(hs + rb(ds));
      if (lane < 32) *(GAS unsigned short*)(out + (b7 * 1024 + rcol + 8 * rr) * 2) = __builtin_bit_cast(unsigned short, res);
      TS(11);
    }
    TS_OUT();
  } else if (w < 8) {
    const int a = w - 4;
    if (a == 0) *(LAS unsigned*)(L + L_CTL + 4 * lane) = 0u;
    if (a == 3) {
      const u64 ep = __hip_atomic_load((GAS u64*)(ws + WS_EPOCH), __ATOMIC_RELAXED, __HIP_MEMORY_SCOPE_AGENT);
      const unsigned qo = QP + (kg * 32 + (cbq >> 1)) * 2048 + ksq * 512 + (cbq & 1) * 256 + c15 * 16;
      ST128(empty4, qo);
      ST128(empty4, qo + 4 * 32 * 2048);
      START_BARRIER();
      VM0();
      const u64 tag = ep + 1;
      if (lane == 0) __hip_atomic_store((GAS u64*)(ws + WS_F1) + xcd * 32 + idx, tag, __ATOMIC_RELAXED, __HIP_MEMORY_SCOPE_AGENT);
      *(volatile LAS u64*)(L + L_CTL + F_TAG) = tag;
      arrive(L, F_PZ);
      TS(1);
    } else if (a == 2) {
      const u8* p = (const u8*)(pc0 & ~127ull) + lane * 128;
      unsigned s = 0;
#pragma unroll
      for (int i = 0; i < (CODE_LINES + 63) / 64; ++i) if (i * 64 + lane < CODE_LINES) s ^= *(const GAS unsigned*)(p + i * 8192);
      START_BARRIER();
      asm volatile("" :: "v"(s));
    } else {
      START_BARRIER();
    }
    const int sp = idx & 7;
    const size_t kvoff = ((size_t)bg * 4096 + 3072 + sp * 128 + a * 32) * 256;
    v4u K[8], R[8];
    {
      const __amdgpu_buffer_rsrc_t rw = TILE(kc + kvoff, 8192);
      const unsigned lo = (unsigned)c15 * 256u + kg * 16;
      waitf<1, 1>(L, F_TOK + 4 + a - DEPTH);
#pragma unroll
      for (int j = 0; j < 8; ++j) K[j] = LDW4(rw, lo + (j & 3) * 64, (j >> 2) * 4096, K_NT);
      SB();
    }
    VM0();
    arrive(L, F_TOK + 4 + a);
    TS(3);
    {
      const __amdgpu_buffer_rsrc_t rw = TILE(vc + kvoff, 8192);
      const unsigned lo = (unsigned)(4 * kg) * 256u + c15 * 16;
      waitf<1, 1>(L, F_TOK + 8 + a - DEPTH);
#pragma unroll
      for (int j = 0; j < 8; ++j) R[j] = LDW4(rw, lo + (j & 3) * 256, (j >> 2) * 4096, W_NT);
      SB();
    }
    PRIO(3);
    v4u P = {0u, 0u, 0u, 0u};
    bool done = false;
#pragma nounroll
    for (int step = 0; step < 2; ++step) {
      if (step == 1) {
        VM0();
        arrive(L, F_TOK + 8 + a);
        TS(4);
        if (!done) waitf<8, 1>(L, F_QF);
      }
      if (!done && (step == 1 || peek<8>(L, F_QF))) {
        CBAR();
        f32x4 s0 = zero4, s1 = zero4;
#pragma unroll
        for (int j = 0; j < 4; ++j) {
          const v4u qf = *(LAS v4u*)(L + L_QF + j * 512 + kg * 128 + b7 * 16);
          s0 = mfma(K[j], qf, s0);
          s1 = mfma(K[4 + j], qf, s1);
        }
        float sc[8];
#pragma unroll
        for (int r = 0; r < 4; ++r) { sc[r] = rb(rb(s0[r]) * 0.08838834764831845f); sc[4 + r] = rb(rb(s1[r]) * 0.08838834764831845f); }
        float m = fmaxf(fmaxf(fmaxf(sc[0], sc[1]), fmaxf(sc[2], sc[3])), fmaxf(fmaxf(sc[4], sc[5]), fmaxf(sc[6], sc[7])));
        m = x32_max(x16_max(m));
#pragma unroll
        for (int r = 0; r < 8; ++r) sc[r] = __builtin_amdgcn_exp2f((sc[r] - m) * LOG2E);
        float ls = ((sc[0] + sc[1]) + (sc[2] + sc[3])) + ((sc[4] + sc[5]) + (sc[6] + sc[7]));
        ls = x32_sum(x16_sum(ls));
        P = v4u{pk(sc[0], sc[1]), pk(sc[2], sc[3]), pk(sc[4], sc[5]), pk(sc[6], sc[7])};
        if (lane < 8) *(LAS f32x2*)(L + L_STA + lane * 32 + a * 8) = f32x2{m, ls};
        arrive(L, F_S + a);
        done = true;
        TS(5);
      }
    }
    v4u F[8];
    waitf<1, 1>(L, F_GO2);
    waitf<4, 0>(L, F_S);
    float f0, f1, f2, f3;
    {
      const int hd = lane >> 3;
      const f32x4 sa = ld4(L + L_STA + hd * 32), sb = ld4(L + L_STA + hd * 32 + 16);
      tr8(R, F);
      const v4u pl = P & mlo, ph = P & mhi;
      LAS u8* p = L + L_RA + a * 4096 + b7 * 128 + (((kg * 2 + h3) ^ b7) * 16);
#pragma unroll
      for (int i = 0; i < 4; ++i) *(LAS f32x4*)(p + i * 1024) = mfma(F[4 + i], ph, mfma(F[i], pl, zero4));
      arrive(L, F_A + a);
      const float M = fmaxf(fmaxf(sa[0], sa[2]), fmaxf(sb[0], sb[2]));
      f0 = __builtin_amdgcn_exp2f((sa[0] - M) * LOG2E); f1 = __builtin_amdgcn_exp2f((sa[2] - M) * LOG2E);
      f2 = __builtin_amdgcn_exp2f((sb[0] - M) * LOG2E); f3 = __builtin_amdgcn_exp2f((sb[2] - M) * LOG2E);
      const float Ls = (f0 * sa[1] + f1 * sa[3]) + (f2 * sb[1] + f3 * sb[3]);
      const float inv = __builtin_amdgcn_rcpf(Ls);
      f0 *= inv; f1 *= inv; f2 *= inv; f3 *= inv;
      if (a == 0 && b7 == 0) ST64((v2u{nz(asu(M)), nz(asu(Ls))}), ML + ((bg & 3) * 8 + hd) * 512 + ((bg >> 2) * 8 + sp) * 8);
    }
    TS(2);
    waitf<4, 0>(L, F_A);
    {
      const int hd = lane >> 3;
      LAS u8* p = L + L_RA + a * 1024 + hd * 128 + ((b7 ^ hd) * 16);
      const f32x4 v = (ld4(p) * f0 + ld4(p + 4096) * f1) + (ld4(p + 8192) * f2 + ld4(p + 12288) * f3);
      ST64((v2u{nz(pk(v[0], v[1])), nz(pk(v[2], v[3]))}), OP + ((((bg & 3) * 8 + hd) * 8 + (bg >> 2)) * 8 + sp) * 256 + a * 64 + b7 * 8);
    }
    arrive(L, F_APUB + a);
    PRIO(0);
    TS(6);
    if (mlp) {
      const int rbk = u224 >> 3, cbd = u224 & 7;
      {
        const __amdgpu_buffer_rsrc_t rw = TILE(wd + (size_t)(128 * rbk + 32 * a) * 2048 + cbd * 256, 31 * 2048 + 256);
        const unsigned lo = (unsigned)(16 * (kg >> 1) + (kg & 1)) * 2048u + c15 * 16;
        waitf<1, 2>(L, F_TOK + 24 + a - DEPTH);
#pragma unroll
        for (int j = 0; j < 8; ++j) R[j] = LDW4(rw, lo, (4 * (j & 3) + 2 * (j >> 2)) * 2048, W_NT);
        SB();
      }
      VM0();
      arrive(L, F_TOK + 24 + a);
      TS(7);
      tr8(R, F);
      waitf<4, 1>(L, F_GPUB);
      PRIO(3);
      TS(8);
      v2u g0 = {0u, 0u}, g1 = g0, u0 = g0, u1 = g0;
      {
        const unsigned off = GP + (2 * rbk + (a >> 1)) * 2048 + (a & 1) * 512 + lane * 8;
        POLL(g0 = LD64(off); g1 = LD64(off + 1024); u0 = LD64(off + 112 * 1024); u1 = LD64(off + 113 * 1024);
             m_ = mx2(mx2(mx2(mx2(m_, g0), g1), u0), u1));
      }
      TS(9);
      unsigned hv[2];
#pragma unroll
      for (int d = 0; d < 2; ++d) {
        const float ga = rb(lo16(g0[d]) + lo16(g1[d])), gb = rb(hi16(g0[d]) + hi16(g1[d]));
        const float ua = rb(lo16(u0[d]) + lo16(u1[d])), ub = rb(hi16(u0[d]) + hi16(u1[d]));
        const float sa = rb(__builtin_amdgcn_rcpf(1.f + __builtin_amdgcn_exp2f(-ga * LOG2E)));
        const float sb = rb(__builtin_amdgcn_rcpf(1.f + __builtin_amdgcn_exp2f(-gb * LOG2E)));
        hv[d] = pk(sa * ua, sb * ub);
      }
      const v4u bl = {hv[0] & mlo, hv[1] & mlo, ror8z<0x3>(hv[0]), ror8z<0x3>(hv[1])};
      const v4u bh = {ror8z<0xC>(hv[0]), ror8z<0xC>(hv[1]), hv[0] & mhi, hv[1] & mhi};
#pragma unroll
      for (int i = 0; i < 4; ++i) *(LAS f32x4*)(L + L_RD + a * 4096 + i * 1024 + lane * 16) = mfma(F[4 + i], bh, mfma(F[i], bl, zero4));
      arrive(L, F_D + a);
      waitf<4, 0>(L, F_D);
      {
        LAS u8* p = L + L_RD + a * 1024 + lane * 16;
        const f32x4 v = (ld4(p) + ld4(p + 4096)) + (ld4(p + 8192) + ld4(p + 12288));
        ST128(pub4(v), DP + (cbd * 32 + kg * 8 + a + 4 * h3) * 3584 + rbk * 128 + b7 * 16);
      }
      arrive(L, F_DPUB + a);
      TS(10);
    }
    TS_OUT();
  } else {
    const int v = w - 8;
    {
      v2u xv[4];
#pragma unroll
      for (int m = 0; m < 4; ++m) xv[m] = *(const GAS v2u*)(x + (v * 1024 + ((ksq + m) & 3) * 256 + 4 * lane) * 2);
      SB();
      START_BARRIER();
      float e[16];
#pragma unroll
      for (int m = 0; m < 4; ++m) { e[4 * m] = lo16(xv[m][0]); e[4 * m + 1] = hi16(xv[m][0]); e[4 * m + 2] = lo16(xv[m][1]); e[4 * m + 3] = hi16(xv[m][1]); }
      float s = 0.f;
#pragma unroll
      for (int i = 0; i < 16; ++i) s += e[i];
      const float mean = wave_sum(s) * (1.f / 1024.f);
      float q = 0.f;
#pragma unroll
      for (int i = 0; i < 16; ++i) { const float d = e[i] - mean; q = fmaf(d, d, q); }
      const float var = wave_sum(q) * (1.f / 1024.f);
      const float mb = rb(mean), den = rb(sqrtf(rb(rb(var) + 1e-5f)));
      const v2u xn = {pk(rb(e[0] - mb) / den, rb(e[1] - mb) / den), pk(rb(e[2] - mb) / den, rb(e[3] - mb) / den)};
      *(LAS v2u*)(L + L_XN + (lane >> 1) * 128 + v * 16 + (lane & 1) * 8) = xn;
      arrive(L, F_XN + v);
    }
    TS(1);
    u64 tag = 0;
    if (v == 7) {
#pragma unroll
      for (unsigned i = 0; i < POISON_KB; ++i) ST128(empty4, OP + (c * POISON_KB + i) * 1024 + lane * 16);
      waitf<1, 1>(L, F_PZ);
      tag = *(volatile LAS u64*)(L + L_CTL + F_TAG);
      VM0();
      if (lane == 0) __hip_atomic_store((GAS u64*)(ws + WS_F2) + c, tag, __ATOMIC_RELAXED, __HIP_MEMORY_SCOPE_AGENT);
      TS(10);
    } else if (v == 0) {
      waitf<1, 1>(L, F_PZ);
      tag = *(volatile LAS u64*)(L + L_CTL + F_TAG);
      waitf<1, 2>(L, F_TOK + 0);
#pragma nounroll
      for (unsigned it = 0; it < SPIN_G; ++it) {
        const bool ok = __hip_atomic_load((GAS u64*)(ws + WS_F1) + (bg & 3) * 64 + lane, __ATOMIC_RELAXED, __HIP_MEMORY_SCOPE_AGENT) == tag;
        if (__builtin_amdgcn_ballot_w64(!ok) == 0) break;
        __builtin_amdgcn_s_sleep(4);
      }
      arrive(L, F_GO1);
      TS(10);
    }
    {
      waitf<1, 1>(L, F_GO1);
      waitf<2, 1>(L, F_QPUB);
      PRIO(2);
      TS(2);
      const unsigned off = QP + ((bg >> 2) * 32 + (bg & 3) * 8 + v) * 2048 + (lane >> 5) * 1024 + (lane & 31) * 16;
      v4u p0 = empty4, p1 = empty4;
      POLL(p0 = LD128(off); p1 = LD128(off + 512); m_ = mx4(mx4(m_, p0), p1));
      TS(3);
      float qv[4];
#pragma unroll
      for (int r = 0; r < 4; ++r) qv[r] = x32_sum(asf(p0[r]) + asf(p1[r]));
      const bool up = lane >= 32;
      const unsigned pr = pk(up ? qv[2] : qv[0], up ? qv[3] : qv[1]);
      LAS u8* p = L + L_QF + ((lane >> 3) & 3) * 512 + ((lane >> 1) & 1) * 256 + (up ? 128 : 0) + v * 16 + (((lane >> 2) & 1) + 2 * (lane & 1)) * 2;
      *(LAS unsigned short*)(p) = (unsigned short)pr;
      *(LAS unsigned short*)(p + 8) = (unsigned short)(pr >> 16);
      arrive(L, F_QF + v);
      PRIO(0);
    }
    TS(4);
    if (v == 0) {
#pragma nounroll
      for (unsigned it = 0; it < SPIN_G; ++it) {
        bool ok = true;
#pragma unroll
        for (int i = 0; i < 4; ++i) ok &= __hip_atomic_load((GAS u64*)(ws + WS_F2) + i * 64 + lane, __ATOMIC_RELAXED, __HIP_MEMORY_SCOPE_AGENT) == tag;
        if (__builtin_amdgcn_ballot_w64(!ok) == 0) break;
        __builtin_amdgcn_s_sleep(4);
      }
      arrive(L, F_GO2);
      if (c == 0 && lane == 0) __hip_atomic_store((GAS u64*)(ws + WS_EPOCH), tag, __ATOMIC_RELAXED, __HIP_MEMORY_SCOPE_AGENT);
      TS(11);
    }
    if (mlp) {
      const int mat = u224 >= 112, cb = (u224 - mat * 112) >> 1, ks = u224 & 1;
      v4u F[8];
      {
        v2u R[16];
        const __amdgpu_buffer_rsrc_t rw = TILE((mat ? wu : wg) + (size_t)(512 * ks + 64 * v) * 7168 + cb * 128, 63 * 7168 + 128);
        const unsigned lo = (unsigned)(2 * kg) * 7168u + c15 * 8;
        waitf<1, 2>(L, F_TOK + 16 + v - DEPTH);
#pragma unroll
        for (int j = 0; j < 16; ++j) R[j] = LDW2(rw, lo, (32 * (j >> 3) + 8 * (j & 3) + ((j >> 2) & 1)) * 7168, W_NT);
        SB();
        VM0();
        arrive(L, F_TOK + 16 + v);
        TS(5);
        tr4(R, F);
      }
      const v4u ones = {0x3f803f80u, 0x3f803f80u, 0x3f803f80u, 0x3f803f80u};
      f32x4 cs = zero4;
#pragma nounroll
      for (int pass = 0; pass < 2; ++pass) {
        v4u hf = ones, ho = ones;
        if (pass == 1) {
          TS(6);
          waitf<1, 1>(L, F_HPUB);
          PRIO(3);
          TS(7);
          POLL(hf = LD128(HF + (8 * ks + v) * 1024 + lane * 16); ho = LD128(HF + (8 * (1 - ks) + v) * 1024 + lane * 16);
               m_ = mx4(mx4(m_, hf), ho));
          TS(8);
        }
        const v4u l0 = hf & mlo, h1 = hf & mhi;
        const v4u h0 = {ror8z<0xC>(hf[0]), ror8z<0xC>(hf[1]), ror8z<0xC>(hf[2]), ror8z<0xC>(hf[3])};
        const v4u l1 = {ror8z<0x3>(hf[0]), ror8z<0x3>(hf[1]), ror8z<0x3>(hf[2]), ror8z<0x3>(hf[3])};
        const f32x4 sq = mfma(ho, ho, mfma(hf, hf, zero4));
        const f32x4 sm = mfma(ones, ho, mfma(ones, hf, zero4));
        LAS u8* rbase = L + L_RG + pass * 16384;
        LAS u8* sbase = L + L_STG + pass * 1024;
#pragma unroll
        for (int i = 0; i < 2; ++i)
          *(LAS f32x4*)(rbase + v * 2048 + i * 1024 + lane * 16) = mfma(F[4 + i], l1, mfma(F[2 + i], h0, mfma(F[6 + i], h1, mfma(F[i], l0, zero4))));
        {
          const int r = lane & 3;
          const float dq = r == 0 ? sq[0] : r == 1 ? sq[1] : r == 2 ? sq[2] : sq[3];
          if (kg == (c15 >> 2)) *(LAS f32x2*)(sbase + v * 128 + b7 * 16 + h3 * 8) = f32x2{sm[0], dq};
        }
        arrive(L, F_G0 + pass * 8 + v);
        if (v < 4) {
          waitf<8, 0>(L, F_G0 + pass * 8);
          LAS u8* p = rbase + (lane >> 5) * 8192 + (kg & 1) * 1024 + (v * 16 + c15) * 16;
          f32x4 t = (ld4(p) + ld4(p + 2048)) + (ld4(p + 4096) + ld4(p + 6144));
          LAS u8* ps = sbase + (lane >> 5) * 512 + b7 * 16;
          const f32x4 s4 = (ld4(ps) + ld4(ps + 128)) + (ld4(ps + 256) + ld4(ps + 384));
#pragma unroll
          for (int r = 0; r < 4; ++r) t[r] = x32_sum(t[r]);
          if (pass == 0) {
            cs = t;
          } else {
            const float mean = x32_sum(s4[0] + s4[2]) * (1.f / 1024.f);
            const float var = fmaxf(x32_sum(s4[1] + s4[3]) * (1.f / 1024.f) - mean * mean, 0.f);
            const float mb = rb(mean), inv = __builtin_amdgcn_rcpf(rb(__builtin_amdgcn_sqrtf(rb(rb(var) + 1e-5f))));
            const f32x4 gq = (t - cs * mb) * inv;
            if (lane < 32) ST64((v2u{nz(pk(gq[0], gq[1])), nz(pk(gq[2], gq[3]))}), GP + u224 * 1024 + v * 256 + lane * 8);
            arrive(L, F_GPUB + v);
          }
        }
      }
      TS(9);
    }
    TS_OUT();
  }
}

extern "C" void kernel_launch(void* const* d_in, const int* in_sizes, int n_in, void* d_out, int out_size, void* d_ws, size_t ws_size,
                              hipStream_t stream) {
  (void)in_sizes; (void)n_in; (void)out_size; (void)ws_size;
  (void)hipFuncSetAttribute((const void*)fused_decode_kernel, hipFuncAttributeMaxDynamicSharedMemorySize, L_END);
  fused_decode_kernel<<<256, 1024, L_END, stream>>>((const u8*)d_in[3], (const u8*)d_in[0], (u8*)d_ws, (const u8*)d_in[1],
                                                    (const u8*)d_in[2], (const u8*)d_in[4], (const u8*)d_in[6], (const u8*)d_in[7],
                                                    (const u8*)d_in[8], (u8*)d_out);
}
```

```cpp
#include <hip/hip_runtime.h>
#include <stdint.h>

#define GAS __attribute__((address_space(1)))
#define LAS __attribute__((address_space(3)))
typedef unsigned v4u __attribute__((ext_vector_type(4)));
typedef unsigned v2u __attribute__((ext_vector_type(2)));
typedef float f32x4 __attribute__((ext_vector_type(4)));
typedef float f32x2 __attribute__((ext_vector_type(2)));
typedef __bf16 bf16x8 __attribute__((ext_vector_type(8)));
typedef __bf16 bf16x2 __attribute__((ext_vector_type(2)));
typedef unsigned long long u64;
typedef unsigned char u8;

#define W_NT 1
#define K_NT 1
#define KNOB(n, v)
#define KWIN 0
#define KPACE 2
#define VWIN 3
#define T_F1 150
#define T_Q 60
#define T_WO 520
#define D_Q 16
#define VW0 1
#define DRY 1
#define T_F3 560
#define T_GU 680
#define D_DN 0
#define D_H3 12
#define D_H6 12
#define CODE_LINES 120
#define PSLEEP 1

constexpr unsigned WS_EPOCH = 0, WS_F2 = 4096, WS_F3 = 6144, WS_F1 = 8192, ARENA = 65536;
constexpr unsigned QP = ARENA;
constexpr unsigned OP = QP + 512 * 1024;
constexpr unsigned WP = OP + 256 * 1024;
constexpr unsigned DP = WP + 512 * 1024;
constexpr unsigned GP = DP + 512 * 1024;
constexpr unsigned ML = GP + 128 * 1024;
constexpr unsigned HF = ML + 32 * 1024;
constexpr unsigned WS_END = HF + 32 * 1024;
constexpr unsigned WS_STAMP = 16u << 20;

constexpr int L_XN = 0, L_QF = 0, L_STA = 2048, L_GO = 3072, L_STG = 4096;
constexpr int L_P = 8192, L_R0 = 16384, L_R1 = L_R0 + 32768, L_R2 = L_R1 + 32768, L_END = L_R2 + 32768;

constexpr float LOG2E = 1.4426950408889634f;
constexpr unsigned SPIN_G = 1u << 16, SPIN_L = 1u << 22;

__device__ __forceinline__ float asf(unsigned u) { return __builtin_bit_cast(float, u); }
__device__ __forceinline__ unsigned asu(float f) { return __builtin_bit_cast(unsigned, f); }
__device__ __forceinline__ float lo16(unsigned u) { return asf(u << 16); }
__device__ __forceinline__ float hi16(unsigned u) { return asf(u & 0xffff0000u); }
__device__ __forceinline__ unsigned pk(float a, float b) { bf16x2 v = {(__bf16)a, (__bf16)b}; return __builtin_bit_cast(unsigned, v); }
__device__ __forceinline__ float rb(float a) { return (float)(__bf16)a; }
__device__ __forceinline__ unsigned nz(unsigned u) { return u < 0xfffffffeu ? u : 0xfffffffeu; }
template <int CTRL> __device__ __forceinline__ float dpp(float x) {
  return __builtin_bit_cast(float, __builtin_amdgcn_mov_dpp(__builtin_bit_cast(int, x), CTRL, 0xf, 0xf, true));
}
template <int BANK> __device__ __forceinline__ unsigned ror8z(unsigned x) {
  return (unsigned)__builtin_amdgcn_update_dpp(0, (int)x, 0x128, 0xf, BANK, false);
}
__device__ __forceinline__ float x32_sum(float x) { auto t = __builtin_amdgcn_permlane32_swap(asu(x), asu(x), false, false); return asf(t[0]) + asf(t[1]); }
__device__ __forceinline__ float x16_sum(float x) { auto t = __builtin_amdgcn_permlane16_swap(asu(x), asu(x), false, false); return asf(t[0]) + asf(t[1]); }
__device__ __forceinline__ float x32_max(float x) { auto t = __builtin_amdgcn_permlane32_swap(asu(x), asu(x), false, false); return fmaxf(asf(t[0]), asf(t[1])); }
__device__ __forceinline__ float x16_max(float x) { auto t = __builtin_amdgcn_permlane16_swap(asu(x), asu(x), false, false); return fmaxf(asf(t[0]), asf(t[1])); }
__device__ __forceinline__ float wave_sum(float x) {
  x += dpp<0xB1>(x); x += dpp<0x4E>(x); x += dpp<0x141>(x); x += dpp<0x128>(x);
  return x32_sum(x16_sum(x));
}
__device__ __forceinline__ f32x4 mfma(v4u a, v4u b, f32x4 c) {
  return __builtin_amdgcn_mfma_f32_16x16x32_bf16(__builtin_bit_cast(bf16x8, a), __builtin_bit_cast(bf16x8, b), c, 0, 0, 0);
}
#define CBAR() asm volatile("" ::: "memory")
#define VM0() asm volatile("s_waitcnt vmcnt(0)" ::: "memory")
#define SB() __builtin_amdgcn_sched_barrier(0)
#define PRIO(n) __builtin_amdgcn_s_setprio(n)
#define TILE(p, bytes) __builtin_amdgcn_make_buffer_rsrc((void*)(p), 0, (int)(bytes), 0x00020000)
#define LDW4(rw, lo, so, NT) __builtin_amdgcn_raw_buffer_load_b128(rw, (int)(lo), (int)(so), (NT) ? 2 : 0)
#define LDW2(rw, lo, so, NT) __builtin_amdgcn_raw_buffer_load_b64(rw, (int)(lo), (int)(so), (NT) ? 2 : 0)

__device__ __forceinline__ void tr8(const v4u (&R)[8], v4u (&F)[8]) {
#pragma unroll
  for (int d = 0; d < 4; ++d)
#pragma unroll
    for (int m = 0; m < 4; ++m) {
      F[2 * d][m] = __builtin_amdgcn_perm(R[2 * m + 1][d], R[2 * m][d], 0x05040100u);
      F[2 * d + 1][m] = __builtin_amdgcn_perm(R[2 * m + 1][d], R[2 * m][d], 0x07060302u);
    }
}
__device__ __forceinline__ void tr4(const v2u (&R)[16], v4u (&F)[8]) {
#pragma unroll
  for (int t = 0; t < 2; ++t)
#pragma unroll
    for (int d = 0; d < 2; ++d)
#pragma unroll
      for (int m = 0; m < 4; ++m) {
        F[4 * t + 2 * d][m] = __builtin_amdgcn_perm(R[8 * t + 2 * m + 1][d], R[8 * t + 2 * m][d], 0x05040100u);
        F[4 * t + 2 * d + 1][m] = __builtin_amdgcn_perm(R[8 * t + 2 * m + 1][d], R[8 * t + 2 * m][d], 0x07060302u);
      }
}
__device__ __forceinline__ unsigned mx4(unsigned m, v4u a) { return max(max(max(m, a[0]), max(a[1], a[2])), a[3]); }
__device__ __forceinline__ unsigned mx2(unsigned m, v2u a) { return max(max(m, a[0]), a[1]); }
#define LD128(off) __builtin_amdgcn_raw_buffer_load_b128(rs, (int)(off), 0, 16)
#define LD64(off) __builtin_amdgcn_raw_buffer_load_b64(rs, (int)(off), 0, 16)
#define ST128(v, off) __builtin_amdgcn_raw_buffer_store_b128(v, rs, (int)(off), 0, 16)
#define ST64(v, off) __builtin_amdgcn_raw_buffer_store_b64(v, rs, (int)(off), 0, 16)
__device__ __forceinline__ v4u pub4(f32x4 v) { return v4u{nz(asu(v[0])), nz(asu(v[1])), nz(asu(v[2])), nz(asu(v[3]))}; }
__device__ __forceinline__ f32x4 ld4(LAS u8* p) { return *(LAS f32x4*)p; }

#define TS_DECL
#define TS(i)
#define TS_OUT()

#define BAR() do { asm volatile("s_waitcnt lgkmcnt(0)" ::: "memory"); __builtin_amdgcn_s_barrier(); CBAR(); } while (0)
#define PIN(x) asm volatile("" : "+v"(x))
#define PINS(x) asm volatile("" : "+s"(x))
#define PIN8(F) asm volatile("" : "+v"(F[0]), "+v"(F[1]), "+v"(F[2]), "+v"(F[3]), "+v"(F[4]), "+v"(F[5]), "+v"(F[6]), "+v"(F[7]))
#define ALD64(off) __hip_atomic_load((GAS u64*)(ws + (off)), __ATOMIC_RELAXED, __HIP_MEMORY_SCOPE_AGENT)
#define AST64(off, v) __hip_atomic_store((GAS u64*)(ws + (off)), v, __ATOMIC_RELAXED, __HIP_MEMORY_SCOPE_AGENT)
template <int N> __device__ __forceinline__ void vmw() { asm volatile("s_waitcnt vmcnt(%0)" :: "i"(N) : "memory"); }
template <int N> __device__ __forceinline__ void nap() { if (N > 0) __builtin_amdgcn_s_sleep(N); }
__device__ __forceinline__ bool bad4(v4u a) { return max(max(a[0], a[1]), max(a[2], a[3])) == 0xffffffffu; }
__device__ __forceinline__ bool bad2(v2u a) { return max(a[0], a[1]) == 0xffffffffu; }
__device__ __forceinline__ bool any(bool p) { return __builtin_amdgcn_ballot_w64(p) != 0; }
__device__ __forceinline__ float qdiv(float a, float d, float inv) { const float q = a * inv; return fmaf(fmaf(-q, d, a), inv, q); }
__device__ __forceinline__ void until(u64 t0, unsigned T) {
#pragma nounroll
  for (int it = 0; it < 4096; ++it) {
    if ((unsigned)(__builtin_amdgcn_s_memrealtime() - t0) >= T) break;
    __builtin_amdgcn_s_sleep(1);
  }
}
__device__ __forceinline__ bool early(LAS u8* L) { return __builtin_amdgcn_readfirstlane(*(volatile LAS unsigned*)(L + L_GO)) == 0; }
__device__ __forceinline__ void setf(LAS u8* L, int off) { CBAR(); *(volatile LAS u8*)(L + off) = 1; CBAR(); }
__device__ __forceinline__ void wait8(LAS u8* L, int off) {
#pragma nounroll
  for (unsigned it = 0; it < SPIN_L; ++it) {
    const v2u v = *(volatile LAS v2u*)(L + off);
    if (__builtin_amdgcn_readfirstlane(v[0] & v[1]) == 0x01010101) break;
  }
  CBAR();
}
#define BL4 "buffer_load_dwordx4 "
#define BL2 "buffer_load_dwordx2 "
#define EXM "s_mov_b64 exec, "
#define SWEND "s_mov_b64 exec, -1\n\ts_waitcnt vmcnt(0)"
#define BAL(p) __builtin_amdgcn_ballot_w64(p)
#define SPIN(body) _Pragma("nounroll") for (unsigned it_ = 0;; ++it_) { body; if (it_ >= SPIN_G) break; nap<PSLEEP>(); }
__device__ __forceinline__ void get1x4(v4u rsv, unsigned off, v4u& a) {
  u64 m0 = ~0ull;
  SPIN(asm volatile(EXM "%1\n\t" BL4 "%0, %2, %3, 0 offen sc1\n\t" SWEND : "+v"(a) : "s"(m0), "v"(off), "s"(rsv) : "memory");
       m0 = BAL(bad4(a)); if (!m0) break)
}
__device__ __forceinline__ void get2x4(v4u rsv, unsigned off, v4u& a, v4u& b) {
  u64 m0 = ~0ull, m1 = ~0ull;
  SPIN(asm volatile(EXM "%2\n\t" BL4 "%0, %4, %5, 0 offen sc1\n\t" EXM "%3\n\t" BL4 "%1, %4, %5, 0 offen offset:1024 sc1\n\t" SWEND
                    : "+v"(a), "+v"(b) : "s"(m0), "s"(m1), "v"(off), "s"(rsv) : "memory");
       m0 = BAL(bad4(a)); m1 = BAL(bad4(b)); if (!(m0 | m1)) break)
}
__device__ __forceinline__ void get4x4(v4u rsv, unsigned off, v4u (&a)[4]) {
  u64 m0 = ~0ull, m1 = ~0ull, m2 = ~0ull, m3 = ~0ull;
  SPIN(asm volatile(EXM "%4\n\t" BL4 "%0, %8, %9, 0 offen sc1\n\t" EXM "%5\n\t" BL4 "%1, %8, %9, 0 offen offset:1024 sc1\n\t"
                    EXM "%6\n\t" BL4 "%2, %8, %9, 0 offen offset:2048 sc1\n\t" EXM "%7\n\t" BL4 "%3, %8, %9, 0 offen offset:3072 sc1\n\t" SWEND
                    : "+v"(a[0]), "+v"(a[1]), "+v"(a[2]), "+v"(a[3]) : "s"(m0), "s"(m1), "s"(m2), "s"(m3), "v"(off), "s"(rsv) : "memory");
       m0 = BAL(bad4(a[0])); m1 = BAL(bad4(a[1])); m2 = BAL(bad4(a[2])); m3 = BAL(bad4(a[3])); if (!((m0 | m1) | (m2 | m3))) break)
}
__device__ __forceinline__ void get2x2(v4u rsv, unsigned o0, unsigned o1, v2u& a, v2u& b) {
  u64 m0 = ~0ull, m1 = ~0ull;
  SPIN(asm volatile(EXM "%2\n\t" BL2 "%0, %4, %6, 0 offen sc1\n\t" EXM "%3\n\t" BL2 "%1, %5, %6, 0 offen sc1\n\t" SWEND
                    : "+v"(a), "+v"(b) : "s"(m0), "s"(m1), "v"(o0), "v"(o1), "s"(rsv) : "memory");
       m0 = BAL(bad2(a)); m1 = BAL(bad2(b)); if (!(m0 | m1)) break)
}
__device__ __forceinline__ void get5x2(v4u rsv, unsigned om, unsigned off, v2u& z, v2u (&a)[4]) {
  u64 mz = ~0ull, m0 = ~0ull, m1 = ~0ull, m2 = ~0ull, m3 = ~0ull;
  SPIN(asm volatile(EXM "%5\n\t" BL2 "%4, %10, %12, 0 offen sc1\n\t" EXM "%6\n\t" BL2 "%0, %11, %12, 0 offen sc1\n\t"
                    EXM "%7\n\t" BL2 "%1, %11, %12, 0 offen offset:256 sc1\n\t" EXM "%8\n\t" BL2 "%2, %11, %12, 0 offen offset:512 sc1\n\t"
                    EXM "%9\n\t" BL2 "%3, %11, %12, 0 offen offset:768 sc1\n\t" SWEND
                    : "+v"(a[0]), "+v"(a[1]), "+v"(a[2]), "+v"(a[3]), "+v"(z)
                    : "s"(mz), "s"(m0), "s"(m1), "s"(m2), "s"(m3), "v"(om), "v"(off), "s"(rsv) : "memory");
       mz = BAL(bad2(z)); m0 = BAL(bad2(a[0])); m1 = BAL(bad2(a[1])); m2 = BAL(bad2(a[2])); m3 = BAL(bad2(a[3]));
       if (!((mz | m0 | m1) | (m2 | m3))) break)
}
#define FLAGS_WAIT(F, sl) _Pragma("nounroll") for (unsigned it_ = 0; it_ < SPIN_G; ++it_) { \
    const u64 fa_ = ALD64(F + lane * 8), fb_ = ALD64(F + 512 + lane * 8); \
    if (!any(((fa_ ^ tag) | (fb_ ^ tag)) != 0)) break; __builtin_amdgcn_s_sleep(sl); }

__global__ void __launch_bounds__(1024, 4)
fused_decode_kernel(const u8* __restrict__ wq, const u8* __restrict__ x, u8* ws, const u8* __restrict__ kc,
                    const u8* __restrict__ vc, const u8* __restrict__ wo, const u8* __restrict__ wg, const u8* __restrict__ wu,
                    const u8* __restrict__ wd, u8* __restrict__ out) {
  extern __shared__ __attribute__((aligned(16))) u8 smem[];
  LAS u8* L = (LAS u8*)smem;
  const u64 t0 = __builtin_amdgcn_s_memrealtime();
  u64 pc0;
  asm volatile("s_getpc_b64 %0" : "=s"(pc0));
  const int c = blockIdx.x, xcd = c & 7, i16 = (c >> 3) & 15, u = xcd * 16 + i16;
  const bool isx = c < 128;
  const int tid = threadIdx.x, lane = tid & 63, w = __builtin_amdgcn_readfirstlane(tid >> 6) ^ (isx ? 0 : 8);
  TS_DECL;
  TS(0);
  unsigned pf = 0;
  if (!isx && w < 3) {
    const unsigned ln = min(tid - 8 * 64, CODE_LINES - 1);
    asm volatile("global_load_dword %0, %1, %2" : "=v"(pf) : "v"(ln * 128), "s"(pc0 & ~127ull) : "memory");
  }
  asm volatile("" :: "s"(wq), "s"(x), "s"(ws), "s"(kc));
  const __amdgpu_buffer_rsrc_t rs = __builtin_amdgcn_make_buffer_rsrc((void*)ws, 0, (int)WS_END, 0x00020000);
  const v4u rsv = {(unsigned)(u64)ws, (unsigned)((u64)ws >> 32) & 0xffffu, WS_END, 0x00020000u};
  const int b7 = lane & 7, h3 = (lane >> 3) & 1, kg = lane >> 4, c15 = lane & 15;
  unsigned mlo = h3 ? 0u : 0xffffffffu, mhi = ~mlo;
  const f32x4 zero4 = {0.f, 0.f, 0.f, 0.f};
  const v4u empty4 = {0xffffffffu, 0xffffffffu, 0xffffffffu, 0xffffffffu};
  const int hq = i16 * 2 + (xcd >> 2), ksq = xcd & 3;

  if (__builtin_expect(isx, 1)) {
  v4u R[8];
  u64 ep0 = 0;
  if (w >= 8) {
    PRIO(3);
    const __amdgpu_buffer_rsrc_t rx = TILE(x, 16384);
    R[0] = LDW4(rx, lane * 16, (w - 8) * 2048, 0);
    R[1] = LDW4(rx, lane * 16 + 1024, (w - 8) * 2048, 0);
    __builtin_amdgcn_raw_buffer_store_b64((v2u{0xffffffffu, 0xffffffffu}), rs, lane * 8, QP + (w - 8) * 65536 + (hq * 4 + ksq) * 512, 16);
    ep0 = ALD64(WS_EPOCH);
    SB();
  } else {
    const __amdgpu_buffer_rsrc_t rw = TILE(wq + (size_t)(256 * ksq + 32 * w) * 8192 + hq * 256, 31 * 8192 + 256);
    const unsigned lo = (unsigned)(kg * 8) * 8192u + c15 * 16;
#pragma unroll
    for (int j = 0; j < 8; ++j) R[j] = LDW4(rw, lo, j * 8192, W_NT);
    SB();
  }
  if (__builtin_expect(w < 8, 1)) {
    unsigned qoff = QP + w * 65536 + (hq * 4 + ksq) * 512 + (lane & 31) * 16;
    v4u F[8];
    {
      TS(4);
      BAR();
      TS(1);
      const v4u x0 = *(LAS v4u*)(L + L_XN + w * 512 + kg * 128 + ((b7 ^ ((w & 1) * 4 + kg)) * 16));
      v4u bl = x0 & mlo, bh = x0 & mhi;
      unsigned wa[4];
#pragma unroll
      for (int i = 0; i < 4; ++i) { wa[i] = L_R0 + w * 4096 + b7 * 512 + kg * 128 + (((i + 4 * h3) ^ b7) * 16); PIN(wa[i]); }
      unsigned ra = L_R0 + (lane >> 5) * 16384 + w * 512 + ((lane >> 3) & 3) * 128 + ((b7 ^ w) * 16);
      PIN(bl); PIN(bh); PIN(ra); PIN(qoff);
      tr8(R, F);
      TS(2);
#pragma unroll
      for (int i = 0; i < 4; ++i) *(LAS f32x4*)(L + wa[i]) = mfma(F[4 + i], bh, mfma(F[i], bl, zero4));
      BAR();
      f32x4 v = (ld4(L + ra) + ld4(L + ra + 4096)) + (ld4(L + ra + 8192) + ld4(L + ra + 12288));
#pragma unroll
      for (int r = 0; r < 4; ++r) v[r] = x32_sum(v[r]);
      if (lane < 32) ST128(pub4(v), qoff);
    }
    TS(3);
    if (w < 7) {
      const int rbk = u >> 3, cbd = u & 7, hb = 7 * rbk + w;
      const __amdgpu_buffer_rsrc_t rw = TILE(wd + (size_t)(32 * hb) * 2048 + cbd * 256, 31 * 2048 + 256);
      unsigned lo = (unsigned)(4 * (kg >> 1) + (kg & 1)) * 2048u + c15 * 16;
      unsigned goff = GP + hb * 512 + lane * 8;
      unsigned wa = L_R2 + w * 4096 + lane * 16, ra = L_R2 + w * 1024 + lane * 16;
      unsigned doff = DP + (cbd * 16 + kg * 4 + w) * 4096 + rbk * 256 + c15 * 16;
      unsigned uoff = goff + 57344;
      v2u gv = {0u, 0u}, uv = {0u, 0u};
      PIN(lo); PIN(goff); PIN(uoff); PIN(wa); PIN(ra); PIN(doff); PIN(mlo); PIN(mhi); PIN(gv); PIN(uv);
      BAR(); BAR(); BAR();
      BAR();
      nap<D_DN>();
      {
        v4u R[8];
#pragma unroll
        for (int j = 0; j < 8; ++j) R[j] = LDW4(rw, lo, (8 * (j & 3) + 2 * (j >> 2)) * 2048, W_NT);
        SB();
        VM0();
        TS(7);
        tr8(R, F);
        PIN8(F);
      }
      BAR();
      get2x2(rsv, goff, uoff, gv, uv);
      TS(8);
      unsigned hv[2];
#pragma unroll
      for (int d = 0; d < 2; ++d) {
        const float sa = rb(__builtin_amdgcn_rcpf(1.f + __builtin_amdgcn_exp2f(-lo16(gv[d]) * LOG2E)));
        const float sb = rb(__builtin_amdgcn_rcpf(1.f + __builtin_amdgcn_exp2f(-hi16(gv[d]) * LOG2E)));
        hv[d] = pk(sa * lo16(uv[d]), sb * hi16(uv[d]));
      }
      const v4u bl = {hv[0] & mlo, hv[1] & mlo, ror8z<0x3>(hv[0]), ror8z<0x3>(hv[1])};
      const v4u bh = {ror8z<0xC>(hv[0]), ror8z<0xC>(hv[1]), hv[0] & mhi, hv[1] & mhi};
#pragma unroll
      for (int i = 0; i < 4; ++i) *(LAS f32x4*)(L + wa + i * 1024) = mfma(F[4 + i], bh, mfma(F[i], bl, zero4));
      BAR();
      if (w < 4) {
        LAS u8* p = L + ra;
        const f32x4 v = ((ld4(p) + ld4(p + 4096)) + (ld4(p + 8192) + ld4(p + 12288))) + ((ld4(p + 16384) + ld4(p + 20480)) + ld4(p + 24576));
        ST128(pub4(v), doff);
      }
      TS(9);
    } else {
      const int rcol = 128 * (u >> 4) + 32 * ((u >> 2) & 3) + (u & 3) + 4 * h3;
      float xr[4];
#pragma unroll
      for (int r = 0; r < 4; ++r) { xr[r] = lo16(*(const GAS unsigned short*)(x + (b7 * 1024 + rcol + 8 * r) * 2)); PIN(xr[r]); }
      unsigned poff = WP + u * 4096 + lane * 16;
      unsigned hoff = HF + ((u >> 3) * 8 + (u & 3) * 2 + ((u >> 2) & 1)) * 256 + b7 * 16 + h3 * 8;
      unsigned ooff = (b7 * 1024 + rcol + 8 * kg) * 2;
      v4u pr[4] = {empty4, empty4, empty4, empty4};
      const u64 tag = ALD64(WS_EPOCH) + 1;
      {
#pragma unroll
        for (int i = 0; i < 4; ++i) { ST128(empty4, poff + i * 1024); ST128(empty4, poff + (DP - WP) + i * 1024); }
        if (lane < 16) ST64((v2u{0xffffffffu, 0xffffffffu}), hoff);
        VM0();
        if (lane == 0) AST64(WS_F3 + u * 8, tag);
      }
      PIN(poff); PIN(hoff); PIN(ooff); PIN(pr[0]); PIN(pr[1]); PIN(pr[2]); PIN(pr[3]);
      BAR();
      until(t0, T_F3);
      FLAGS_WAIT(WS_F3, 4)
      TS(7);
      BAR(); BAR();
      nap<D_H3>();
      TS(4);
      get4x4(rsv, poff, pr);
      TS(5);
      float h4[4];
#pragma unroll
      for (int r = 0; r < 4; ++r) {
        const float s = x32_sum(x16_sum((asf(pr[0][r]) + asf(pr[1][r])) + (asf(pr[2][r]) + asf(pr[3][r]))));
        h4[r] = rb(xr[r] + rb(s));
      }
      if (lane < 16) ST64((v2u{nz(pk(h4[0], h4[1])), nz(pk(h4[2], h4[3]))}), hoff);
      BAR();
      TS(6);
      float hs = kg == 0 ? h4[0] : kg == 1 ? h4[1] : kg == 2 ? h4[2] : h4[3];
      poff += DP - WP;
      PIN(hs); PIN(poff);
      BAR(); BAR();
      nap<D_H6>();
      TS(9);
      get4x4(rsv, poff, pr);
      TS(10);
      float d4[4];
#pragma unroll
      for (int r = 0; r < 4; ++r) d4[r] = x32_sum(x16_sum((asf(pr[0][r]) + asf(pr[1][r])) + (asf(pr[2][r]) + asf(pr[3][r]))));
      const float ds = kg == 0 ? d4[0] : kg == 1 ? d4[1] : kg == 2 ? d4[2] : d4[3];
      const __bf16 res = (__bf16)(hs + rb(ds));
      *(GAS unsigned short*)(out + ooff) = __builtin_bit_cast(unsigned short, res);
      if (c == 0 && lane == 0) AST64(WS_EPOCH, tag);
      TS(11);
    }
    TS_OUT();
  } else {
    const int v = w - 8, hp = u >> 3, cbo = u & 7, ho = 2 * hp + (v >> 2), pw = v & 3;
    VM0();
    PIN(ep0);
    const u64 tag = ep0 + 1;
    if (lane == 0) AST64(WS_F1 + (v * 128 + hq * 4 + ksq) * 8, tag);
    TS(8);
    {
      float e[16];
#pragma unroll
      for (int m = 0; m < 4; ++m) { e[2 * m] = lo16(R[0][m]); e[2 * m + 1] = hi16(R[0][m]); e[8 + 2 * m] = lo16(R[1][m]); e[9 + 2 * m] = hi16(R[1][m]); }
      float s = 0.f;
#pragma unroll
      for (int i = 0; i < 16; ++i) s += e[i];
      const float mean = wave_sum(s) * (1.f / 1024.f);
      float q = 0.f;
#pragma unroll
      for (int i = 0; i < 16; ++i) { const float d = e[i] - mean; q = fmaf(d, d, q); }
      const float var = wave_sum(q) * (1.f / 1024.f);
      const float mb = rb(mean), den = rb(sqrtf(rb(rb(var) + 1e-5f)));
      const float inv = 1.f / den;
      const unsigned hsel = ksq & 2 ? 0xffffffffu : 0u;
      v4u xn;
#pragma unroll
      for (int m = 0; m < 4; ++m) {
        const unsigned xs = (R[0][m] & ~hsel) | (R[1][m] & hsel);
        const float ea = lo16(xs), eb = hi16(xs);
        xn[m] = pk(qdiv(rb(ea - mb), den, inv), qdiv(rb(eb - mb), den, inv));
      }
      if ((lane >> 5) == (ksq & 1)) *(LAS v4u*)(L + L_XN + (lane & 31) * 128 + ((v ^ b7) * 16)) = xn;
      TS(7);
      BAR();
    }
    const __amdgpu_buffer_rsrc_t rw = TILE(wo + (size_t)(128 * ho + pw) * 2048 + cbo * 256, 124 * 2048 + 256);
    unsigned lo = (unsigned)(32 * kg) * 2048u + c15 * 16;
    unsigned moff = ML + ((b7 * 4 + (ho >> 3)) * 4 + kg) * 256 + (ho & 7) * 8;
    unsigned ooff = OP + (ho * 8 + b7) * 1024 + pw * 64 + kg * 16 + h3 * 8;
    const int l2 = (v >> 2) * 32 + (lane & 31);
    unsigned wa = L_R1 + v * 4096 + lane * 16, ra = L_R1 + (lane >> 5) * 16384 + pw * 1024 + l2 * 16;
    unsigned poff = WP + (cbo * 16 + (l2 >> 4) * 4 + pw) * 4096 + hp * 256 + c15 * 16;
    v2u ml = {0u, 0u}, o[4] = {ml, ml, ml, ml};
    PIN(lo); PIN(moff); PIN(ooff); PIN(wa); PIN(ra); PIN(poff); PIN(mlo); PIN(mhi); PIN(ml); PIN(o[0]); PIN(o[1]); PIN(o[2]); PIN(o[3]);
    BAR();
    if (v == 0) {
      until(t0, T_WO - 70);
      FLAGS_WAIT(WS_F2, 4)
      TS(1);
      until(t0, T_WO);
    }
    BAR();
    TS(2);
    v4u F[8];
    {
      v4u R[8];
#pragma unroll
      for (int j = 0; j < 8; ++j) R[j] = LDW4(rw, lo, (8 * (j & 3) + 4 * (j >> 2)) * 2048, W_NT);
      SB();
      VM0();
      TS(3);
      tr8(R, F);
      PIN8(F);
    }
    BAR();
    TS(4);
    {
      get5x2(rsv, moff, ooff, ml, o);
      TS(5);
      const float ms = asf(ml[0]);
      const float M = x32_max(x16_max(ms));
      const float wg_ = __builtin_amdgcn_exp2f((ms - M) * LOG2E) * asf(ml[1]);
      auto t = __builtin_amdgcn_permlane32_swap(asu(wg_), asu(wg_), false, false);
      auto a01 = __builtin_amdgcn_permlane16_swap(t[0], t[0], false, false);
      auto a23 = __builtin_amdgcn_permlane16_swap(t[1], t[1], false, false);
      const float w0 = asf(a01[0]), w1 = asf(a01[1]), w2 = asf(a23[0]), w3 = asf(a23[1]);
      const float inv = __builtin_amdgcn_rcpf((w0 + w1) + (w2 + w3));
      unsigned hv[2];
#pragma unroll
      for (int d = 0; d < 2; ++d) {
        const float a = (w0 * lo16(o[0][d]) + w1 * lo16(o[1][d])) + (w2 * lo16(o[2][d]) + w3 * lo16(o[3][d]));
        const float bq = (w0 * hi16(o[0][d]) + w1 * hi16(o[1][d])) + (w2 * hi16(o[2][d]) + w3 * hi16(o[3][d]));
        hv[d] = pk(a * inv, bq * inv);
      }
      const v4u bl = {hv[0] & mlo, hv[1] & mlo, ror8z<0x3>(hv[0]), ror8z<0x3>(hv[1])};
      const v4u bh = {ror8z<0xC>(hv[0]), ror8z<0xC>(hv[1]), hv[0] & mhi, hv[1] & mhi};
#pragma unroll
      for (int i = 0; i < 4; ++i) *(LAS f32x4*)(L + wa + i * 1024) = mfma(F[4 + i], bh, mfma(F[i], bl, zero4));
    }
    BAR();
    {
      LAS u8* p = L + ra;
      f32x4 t = (ld4(p) + ld4(p + 4096)) + (ld4(p + 8192) + ld4(p + 12288));
#pragma unroll
      for (int r = 0; r < 4; ++r) t[r] = x32_sum(t[r]);
      if (lane < 32) ST128(pub4(t), poff);
    }
    TS(6);
    TS_OUT();
  }
  } else {
    const int bg = u >> 2, sp = u & 3, a = w & 7;
    const size_t kvoff = ((size_t)bg * 4096 + 3072 + sp * 256 + a * 32) * 256;
    u64 tag = 0;
    if (w >= 8) {
      PRIO(3);
      u64 ep0 = 0;
      if (a < 2 || a == 7) ep0 = ALD64(WS_EPOCH);
      if (a == 0) {
        const unsigned oo = OP + (((bg & 3) * 8 + kg) * 8 + (bg >> 2)) * 1024 + sp * 256 + c15 * 16;
        ST128(empty4, oo);
        ST128(empty4, oo + 4 * 8192);
        if (i16 < 14) ST128(empty4, GP + (xcd * 14 + i16) * 1024 + lane * 16);
        if (lane < 4) ST128(empty4, ML + u * 256 + lane * 16);
        if (lane < 8) *(volatile LAS unsigned*)(L + L_GO + lane * 4) = 0u;
      }
      v4u K[8];
      const __amdgpu_buffer_rsrc_t rw = TILE(kc + kvoff, 8192);
      const unsigned lo = (unsigned)c15 * 256u + kg * 16;
      K[0] = LDW4(rw, lo, 0, K_NT);
      K[1] = LDW4(rw, lo + 64, 0, K_NT);
      SB();
      BAR();
      VM0();
      PIN(ep0);
      tag = ep0 + 1;
      if (a == 0 && lane == 0) AST64(WS_F2 + u * 8, tag);
      TS(1);
      unsigned qoff = QP + (bg >> 2) * 65536 + ((bg & 3) * 8 + a) * 2048 + lane * 16;
      unsigned qw = L_QF + ((lane >> 3) & 3) * 512 + (lane >> 5) * 256 + a * 16 + b7 * 2;
      unsigned qa = L_QF + kg * 128 + b7 * 16, sw = L_STA + b7 * 64 + a * 8, sr = L_STA + b7 * 64 + kg * 16;
      unsigned pw = L_P + a * 1024 + lane * 16;
      v4u p0 = empty4, p1 = empty4;
      PIN(qoff); PIN(qw); PIN(qa); PIN(sw); PIN(sr); PIN(pw); PIN(p0); PIN(p1);
#pragma unroll
      for (int j = 2; j < 8; ++j) K[j] = empty4;
#pragma nounroll
      for (int pass = (DRY && a == 2) ? 0 : 1; pass < 2; ++pass) {
      if (pass) {
      if (a == 1) {
        until(t0, T_F1);
#pragma nounroll
        for (unsigned it = 0; it < SPIN_G; ++it) {
          if (!any(ALD64(WS_F1 + ((bg >> 2) * 128 + (bg & 3) * 32 + (lane & 31)) * 8) != tag)) break;
          __builtin_amdgcn_s_sleep(1);
        }
        *(volatile LAS unsigned*)(L + L_GO) = (unsigned)__builtin_amdgcn_s_memrealtime() | 1u;
      }
#pragma unroll
      for (int j = 2; j < 8; ++j) {
        K[j] = LDW4(rw, lo + (j & 3) * 64, (j >> 2) * 4096, K_NT);
        SB();
        if ((j & 1) && j < 7) { if (early(L)) vmw<0>(); else vmw<2>(); }
      }
      setf(L, L_GO + 8 + a);
      TS(2);
#pragma nounroll
      for (unsigned it = 0; it < SPIN_L && early(L); ++it) __builtin_amdgcn_s_sleep(1);
      CBAR();
      until((u64)(unsigned)__builtin_amdgcn_readfirstlane(*(volatile LAS unsigned*)(L + L_GO)), T_Q);
      TS(3);
      get2x4(rsv, qoff, p0, p1);
      TS(4);
      }
      {
        float qv[4];
#pragma unroll
        for (int r = 0; r < 4; ++r) qv[r] = x32_sum(asf(p0[r]) + asf(p1[r]));
        const bool up = lane >= 32;
        const unsigned pr = pk(up ? qv[2] : qv[0], up ? qv[3] : qv[1]);
        *(LAS unsigned short*)(L + qw) = (unsigned short)pr;
        *(LAS unsigned short*)(L + qw + 128) = (unsigned short)(pr >> 16);
      }
      if (pass) { setf(L, L_GO + 16 + a); wait8(L, L_GO + 16); }
      TS(5);
      f32x4 s0 = zero4, s1 = zero4;
#pragma unroll
      for (int j = 0; j < 4; ++j) {
        const v4u qf = *(LAS v4u*)(L + qa + j * 512);
        s0 = mfma(K[j], qf, s0);
        s1 = mfma(K[4 + j], qf, s1);
      }
      float sc[4];
#pragma unroll
      for (int r = 0; r < 4; ++r) sc[r] = rb(rb(h3 ? s1[r] : s0[r]) * 0.08838834764831845f);
      float m = fmaxf(fmaxf(sc[0], sc[1]), fmaxf(sc[2], sc[3]));
      m = fmaxf(m, dpp<0x128>(m));
      m = x32_max(x16_max(m));
#pragma unroll
      for (int r = 0; r < 4; ++r) sc[r] = __builtin_amdgcn_exp2f((sc[r] - m) * LOG2E);
      float ls = (sc[0] + sc[1]) + (sc[2] + sc[3]);
      ls += dpp<0x128>(ls);
      ls = x32_sum(x16_sum(ls));
      *(LAS f32x2*)(L + sw) = f32x2{m, ls};
      if (pass) { setf(L, L_GO + 24 + a); wait8(L, L_GO + 24); }
      {
        const f32x4 st = ld4(L + sr);
        const float M = x32_max(x16_max(fmaxf(st[0], st[2])));
        const float Ls = x32_sum(x16_sum(__builtin_amdgcn_exp2f((st[0] - M) * LOG2E) * st[1] + __builtin_amdgcn_exp2f((st[2] - M) * LOG2E) * st[3]));
        const float f = __builtin_amdgcn_exp2f((m - M) * LOG2E) * __builtin_amdgcn_rcpf(Ls);
        const unsigned pa = pk(sc[0] * f, sc[1] * f), pb = pk(sc[2] * f, sc[3] * f);
        const unsigned oa = asu(dpp<0x128>(asf(pa))), ob = asu(dpp<0x128>(asf(pb)));
        *(LAS v4u*)(L + pw) = h3 ? v4u{oa, ob, pa, pb} : v4u{pa, pb, oa, ob};
        if (a == 0 && lane < 8) ST64((v2u{nz(asu(M)), nz(asu(Ls))}), ML + u * 256 + lane * 8);
      }
      }
      PRIO(0);
      TS(6);
      BAR();
      BAR();
    } else {
      BAR();
      v4u R[8];
      const __amdgpu_buffer_rsrc_t rw = TILE(vc + kvoff, 8192);
      const unsigned lo = (unsigned)(4 * kg) * 256u + c15 * 16;
      const int hd = (a >> 2) * 4 + ((lane >> 3) & 3);
      unsigned wa = L_R0 + a * 4096 + b7 * 128 + (((kg * 2 + h3) ^ b7) * 16);
      unsigned ra = L_R0 + (lane >> 5) * 16384 + (a & 3) * 1024 + hd * 128 + ((b7 ^ hd) * 16);
      unsigned ooff = OP + (((bg & 3) * 8 + hd) * 8 + (bg >> 2)) * 1024 + sp * 256 + (a & 3) * 64 + b7 * 8;
      unsigned pw = L_P + a * 1024 + lane * 16;
      PIN(wa); PIN(ra); PIN(ooff); PIN(pw); PIN(mlo); PIN(mhi);
#pragma unroll
      for (int j = 0; j < 8; ++j) R[j] = empty4;
#pragma nounroll
      for (int pass = (DRY && a == 4) ? 0 : 1; pass < 2; ++pass) {
      if (pass) {
#pragma nounroll
      for (unsigned it = 0; it < SPIN_L && __builtin_amdgcn_readfirstlane(*(volatile LAS u8*)(L + L_GO + 8 + a)) == 0; ++it) __builtin_amdgcn_s_sleep(2);
      VM0();
      asm volatile("" :: "v"(pf));
      TS(1);
#pragma unroll
      for (int j = 0; j < 8; ++j) {
        R[j] = LDW4(rw, lo + (j & 3) * 256, (j >> 2) * 4096, W_NT);
        SB();
        if (j < 7) { if (__builtin_amdgcn_readfirstlane(*(volatile LAS u8*)(L + L_GO + 16 + a)) == 0) vmw<VW0>(); else vmw<VWIN>(); }
      }
      TS(2);
      BAR();
      TS(3);
      }
      {
        const v4u P = *(LAS v4u*)(L + pw);
        v4u pl = P & mlo, ph = P & mhi;
        PIN(pl); PIN(ph);
        v4u F[8];
        tr8(R, F);
        TS(5);
#pragma unroll
        for (int i = 0; i < 4; ++i) *(LAS f32x4*)(L + wa + i * 1024) = mfma(F[4 + i], ph, mfma(F[i], pl, zero4));
      }
      if (pass) BAR();
      {
        LAS u8* p = L + ra;
        f32x4 t = (ld4(p) + ld4(p + 4096)) + (ld4(p + 8192) + ld4(p + 12288));
#pragma unroll
        for (int r = 0; r < 4; ++r) t[r] = x32_sum(t[r]);
        if (pass && lane < 32) ST64((v2u{nz(pk(t[0], t[1])), nz(pk(t[2], t[3]))}), ooff);
      }
      }
      TS(6);
    }
    if (i16 < 14) {
      const int ug = xcd * 14 + i16, mat = ug >= 56, cb = ug - mat * 56;
      const __amdgpu_buffer_rsrc_t rw = TILE((mat ? wu : wg) + (size_t)(64 * w) * 7168 + cb * 128, 63 * 7168 + 128);
      unsigned lo = (unsigned)(h3 * 32 + kg) * 7168u + b7 * 16;
      PIN(lo);
      const bool slow = ((((unsigned)(u64)(mat ? wu : wg) >> 7) + cb) & 7) == 3;
      BAR();
      if (w == 15) { FLAGS_WAIT(WS_F3, 4) }
      if (!slow) until(t0, T_GU);
      v4u F[8];
      {
        v4u R[8];
#pragma unroll
        for (int j = 0; j < 8; ++j) R[j] = LDW4(rw, lo, (8 * (j & 3) + 4 * (j >> 2)) * 7168, W_NT);
        SB();
        VM0();
        TS(7);
        v2u X[16];
#pragma unroll
        for (int j = 0; j < 8; ++j)
#pragma unroll
          for (int d = 0; d < 2; ++d) {
            X[j][d] = (unsigned)__builtin_amdgcn_update_dpp((int)R[j][d], (int)R[j][d + 2], 0x128, 0xf, 0xC, false);
            X[8 + j][d] = (unsigned)__builtin_amdgcn_update_dpp((int)R[j][d + 2], (int)R[j][d], 0x128, 0xf, 0x3, false);
          }
        tr4(X, F);
        PIN8(F);
      }
      const v4u ones = {0x3f803f80u, 0x3f803f80u, 0x3f803f80u, 0x3f803f80u};
      f32x4 cs = zero4;
      unsigned hoff = HF + (w * 8 + (lane >> 3)) * 256 + b7 * 16;
      unsigned wa = L_R1 + w * 2048 + lane * 16, sw = L_STG + w * 128 + b7 * 16 + h3 * 8;
      unsigned ra = L_R1 + kg * 8192 + (w & 1) * 1024 + ((w >> 1) * 16 + c15) * 16, sr = L_STG + kg * 512 + b7 * 16;
      unsigned goff = GP + ug * 1024 + ((w >> 1) & 1) * 512 + (w >> 2) * 256 + (w & 1) * 128 + lane * 8;
      PIN(hoff); PIN(goff);
#pragma nounroll
      for (int pass = 0; pass < 2; ++pass) {
        v4u hf = ones;
        PIN(wa); PIN(sw); PIN(ra); PIN(sr);
        if (pass == 1) {
          TS(8);
          get1x4(rsv, hoff, hf);
          TS(9);
        }
        const v4u l0 = hf & mlo, h1 = hf & mhi;
        const v4u h0 = {ror8z<0xC>(hf[0]), ror8z<0xC>(hf[1]), ror8z<0xC>(hf[2]), ror8z<0xC>(hf[3])};
        const v4u l1 = {ror8z<0x3>(hf[0]), ror8z<0x3>(hf[1]), ror8z<0x3>(hf[2]), ror8z<0x3>(hf[3])};
        const f32x4 sq = mfma(hf, hf, zero4);
        const f32x4 sm = mfma(ones, hf, zero4);
#pragma unroll
        for (int i = 0; i < 2; ++i)
          *(LAS f32x4*)(L + wa + i * 1024) = mfma(F[4 + i], l1, mfma(F[2 + i], h0, mfma(F[6 + i], h1, mfma(F[i], l0, zero4))));
        {
          const int r = lane & 3;
          const float dq = r == 0 ? sq[0] : r == 1 ? sq[1] : r == 2 ? sq[2] : sq[3];
          if (kg == (c15 >> 2)) *(LAS f32x2*)(L + sw) = f32x2{sm[0], dq};
        }
        BAR();
        if (w < 8) {
          LAS u8* p = L + ra;
          f32x4 t = (ld4(p) + ld4(p + 2048)) + (ld4(p + 4096) + ld4(p + 6144));
          LAS u8* ps = L + sr;
          const f32x4 s4 = (ld4(ps) + ld4(ps + 128)) + (ld4(ps + 256) + ld4(ps + 384));
#pragma unroll
          for (int r = 0; r < 4; ++r) t[r] = x32_sum(x16_sum(t[r]));
          const float mean = x32_sum(x16_sum(s4[0] + s4[2])) * (1.f / 1024.f);
          const float var = fmaxf(x32_sum(x16_sum(s4[1] + s4[3])) * (1.f / 1024.f) - mean * mean, 0.f);
          const float mb = rb(mean), inv = __builtin_amdgcn_rcpf(rb(__builtin_amdgcn_sqrtf(rb(rb(var) + 1e-5f))));
          const f32x4 gq = (t - cs * mb) * inv;
          const v2u gp = {nz(pk(gq[0], gq[1])), nz(pk(gq[2], gq[3]))};
          if (pass == 1 && lane < 16) ST64(gp, goff);
          cs = t;
        }
        wa += 32768; ra += 32768; sw += 2048; sr += 2048;
      }
      TS(10);
    }
    TS_OUT();
  }
}

extern "C" void kernel_launch(void* const* d_in, const int* in_sizes, int n_in, void* d_out, int out_size, void* d_ws, size_t ws_size,
                              hipStream_t stream) {
  (void)in_sizes; (void)n_in; (void)out_size; (void)ws_size;
  (void)hipFuncSetAttribute((const void*)fused_decode_kernel, hipFuncAttributeMaxDynamicSharedMemorySize, L_END);
  fused_decode_kernel<<<256, 1024, L_END, stream>>>((const u8*)d_in[3], (const u8*)d_in[0], (u8*)d_ws, (const u8*)d_in[1],
                                                    (const u8*)d_in[2], (const u8*)d_in[4], (const u8*)d_in[6], (const u8*)d_in[7],
                                                    (const u8*)d_in[8], (u8*)d_out);
}
```

```cpp
#include <hip/hip_runtime.h>
#include <stdint.h>

#define GAS __attribute__((address_space(1)))
#define LAS __attribute__((address_space(3)))
typedef unsigned v4u __attribute__((ext_vector_type(4)));
typedef unsigned v2u __attribute__((ext_vector_type(2)));
typedef float f32x4 __attribute__((ext_vector_type(4)));
typedef float f32x2 __attribute__((ext_vector_type(2)));
typedef __bf16 bf16x8 __attribute__((ext_vector_type(8)));
typedef __bf16 bf16x2 __attribute__((ext_vector_type(2)));
typedef unsigned long long u64;
typedef unsigned char u8;

constexpr int T_F1 = 175;
constexpr int T_K1 = 260;
constexpr int T_Q = 140;
constexpr int VEARLY = 3;
constexpr int T_WO = 145;
constexpr int T_WOS = 55;
constexpr int T_F3 = 250;
constexpr int GUE = 3;
constexpr int KE = 4;
constexpr int T_KE = 12;
constexpr int T_FX = 170;
constexpr int WO_N = 3;
constexpr int T_WO2 = 295;
constexpr int T_WOS2 = 20;
constexpr int T_H = 300;
constexpr int T_DN1 = 90;
constexpr int T_DNS = 12;
constexpr int D_H3 = 18;
constexpr int D_H6 = 12;
constexpr int CODE_LINES = 129;

constexpr unsigned WS_EPOCH = 0, WS_F2 = 4096, WS_F1 = 8192, ARENA = 65536;
constexpr unsigned QP = ARENA;
constexpr unsigned OP = QP + 512 * 1024;
constexpr unsigned WP = OP + 256 * 1024;
constexpr unsigned DP = WP + 512 * 1024;
constexpr unsigned GP = DP + 512 * 1024;
constexpr unsigned ML = GP + 128 * 1024;
constexpr unsigned HF = ML + 32 * 1024;
constexpr unsigned WS_END = HF + 32 * 1024;

constexpr int L_XN = 0, L_QF = 0, L_STA = 2048, L_MX = 2560, L_FL = 3072, L_STG = 4096;
constexpr int L_P = 8192, L_R0 = 12288, L_R1 = L_R0 + 32768, L_R2 = L_R1 + 32768, L_END = L_R2 + 32768;

constexpr float LOG2E = 1.4426950408889634f;
constexpr unsigned SPIN_G = 1u << 19, SPIN_L = 1u << 25;

__device__ __forceinline__ float asf(unsigned u) { return __builtin_bit_cast(float, u); }
__device__ __forceinline__ unsigned asu(float f) { return __builtin_bit_cast(unsigned, f); }
__device__ __forceinline__ float lo16(unsigned u) { return asf(u << 16); }
__device__ __forceinline__ float hi16(unsigned u) { return asf(u & 0xffff0000u); }
__device__ __forceinline__ unsigned pk(float a, float b) { bf16x2 v = {(__bf16)a, (__bf16)b}; return __builtin_bit_cast(unsigned, v); }
__device__ __forceinline__ float rb(float a) { return (float)(__bf16)a; }
__device__ __forceinline__ unsigned nz(unsigned u) { return u < 0xfffffffeu ? u : 0xfffffffeu; }
__device__ __forceinline__ float ex2(float a) { return __builtin_amdgcn_exp2f(a); }
__device__ __forceinline__ float rcp(float a) { return __builtin_amdgcn_rcpf(a); }
template <int CTRL> __device__ __forceinline__ float dpp(float x) {
  return __builtin_bit_cast(float, __builtin_amdgcn_mov_dpp(__builtin_bit_cast(int, x), CTRL, 0xf, 0xf, true));
}
__device__ __forceinline__ float t32(float a, float b) { auto t = __builtin_amdgcn_permlane32_swap(asu(a), asu(b), false, false); return asf(t[0]) + asf(t[1]); }
__device__ __forceinline__ float t16(float a, float b) { auto t = __builtin_amdgcn_permlane16_swap(asu(a), asu(b), false, false); return asf(t[0]) + asf(t[1]); }
__device__ __forceinline__ float row_sum(float x) { x += dpp<0xB1>(x); x += dpp<0x4E>(x); x += dpp<0x141>(x); x += dpp<0x128>(x); return x; }
__device__ __forceinline__ f32x4 mfma(v4u a, v4u b, f32x4 c) {
  return __builtin_amdgcn_mfma_f32_16x16x32_bf16(__builtin_bit_cast(bf16x8, a), __builtin_bit_cast(bf16x8, b), c, 0, 0, 0);
}
#define CBAR() asm volatile("" ::: "memory")
#define VM0() asm volatile("s_waitcnt vmcnt(0)" ::: "memory")
#define SB() __builtin_amdgcn_sched_barrier(0)
#define PRIO(n) __builtin_amdgcn_s_setprio(n)
#define BAR() do { asm volatile("s_waitcnt lgkmcnt(0)" ::: "memory"); __builtin_amdgcn_s_barrier(); CBAR(); } while (0)
#define PIN(x) asm volatile("" : "+v"(x))
#define PIN8(F) asm volatile("" : "+v"(F[0]), "+v"(F[1]), "+v"(F[2]), "+v"(F[3]), "+v"(F[4]), "+v"(F[5]), "+v"(F[6]), "+v"(F[7]))
#define TILE(p, bytes) __builtin_amdgcn_make_buffer_rsrc((void*)(p), 0, (int)(bytes), 0x00020000)
#define LDW(rw, lo, so) __builtin_amdgcn_raw_buffer_load_b128(rw, (int)(lo), (int)(so), 2)
#define ST128(v, off) __builtin_amdgcn_raw_buffer_store_b128(v, rs, (int)(off), 0, 16)
#define ST64(v, off) __builtin_amdgcn_raw_buffer_store_b64(v, rs, (int)(off), 0, 16)
#define ST32(v, off) __builtin_amdgcn_raw_buffer_store_b32(v, rs, (int)(off), 0, 16)
#define ALD64(off) __hip_atomic_load((GAS u64*)(ws + (off)), __ATOMIC_RELAXED, __HIP_MEMORY_SCOPE_AGENT)
__device__ __forceinline__ unsigned keep(bool k, unsigned off) { return off | (k ? 0u : 0x80000000u); }
__device__ __forceinline__ unsigned pick(unsigned m, unsigned a, unsigned b) { return (a & m) | (b & ~m); }
#define FLAG(off) ST64((v2u{(unsigned)tag, (unsigned)(tag >> 32)}), keep(lane == 0, off))
template <int N> __device__ __forceinline__ void vmw() { asm volatile("s_waitcnt vmcnt(%0)" :: "i"(N) : "memory"); }
template <int N> __device__ __forceinline__ void nap() { if (N > 0) __builtin_amdgcn_s_sleep(N); }
__device__ __forceinline__ f32x4 ld4(LAS u8* p) { return *(LAS f32x4*)p; }

template <int M0, int M1> __device__ __forceinline__ void trp(const v4u (&R)[8], v4u (&F)[8]) {
#pragma unroll
  for (int m = M0; m < M1; ++m)
#pragma unroll
    for (int d = 0; d < 4; ++d) {
      F[2 * d][m] = __builtin_amdgcn_perm(R[2 * m + 1][d], R[2 * m][d], 0x05040100u);
      F[2 * d + 1][m] = __builtin_amdgcn_perm(R[2 * m + 1][d], R[2 * m][d], 0x07060302u);
    }
}
__device__ __forceinline__ void tr8(const v4u (&R)[8], v4u (&F)[8]) { trp<0, 4>(R, F); }
__device__ __forceinline__ void tr4(const v2u (&R)[16], v4u (&F)[8]) {
#pragma unroll
  for (int t = 0; t < 2; ++t)
#pragma unroll
    for (int d = 0; d < 2; ++d)
#pragma unroll
      for (int m = 0; m < 4; ++m) {
        F[4 * t + 2 * d][m] = __builtin_amdgcn_perm(R[8 * t + 2 * m + 1][d], R[8 * t + 2 * m][d], 0x05040100u);
        F[4 * t + 2 * d + 1][m] = __builtin_amdgcn_perm(R[8 * t + 2 * m + 1][d], R[8 * t + 2 * m][d], 0x07060302u);
      }
}
__device__ __forceinline__ void tile_mma(LAS u8* dst, int step, const v4u (&F)[8], v4u bl, v4u bh) {
  const f32x4 z = {0.f, 0.f, 0.f, 0.f};
  f32x4 a[4];
#pragma unroll
  for (int i = 0; i < 4; ++i) a[i] = mfma(F[i], bl, z);
#pragma unroll
  for (int i = 0; i < 4; ++i) *(LAS f32x4*)(dst + i * step) = mfma(F[4 + i], bh, a[i]);
}
__device__ __forceinline__ f32x2 fold8(LAS u8* p) {
  const f32x4 t = (ld4(p) + ld4(p + 4096)) + (ld4(p + 8192) + ld4(p + 12288));
  return f32x2{t32(t[0], t[2]), t32(t[1], t[3])};
}
template <int N> __device__ __forceinline__ v2u col_mma(LAS u8* e, const v4u (&G)[8], unsigned mlo) {
  v4u bf[4];
#pragma unroll
  for (int k = 0; k < 4; ++k) bf[k] = *(LAS v4u*)(e + k * 1024);
  SB();
  f32x4 a = {0.f, 0.f, 0.f, 0.f}, b = a;
#pragma unroll
  for (int k = 0; k < 4; ++k) { a = mfma(G[2 * k], bf[k], a); if (2 * k + 1 < N) b = mfma(G[2 * k + 1], bf[k], b); }
  const float r0 = asf(pick(mlo, asu(a[0]), asu(b[2]))) + dpp<0x128>(asf(pick(mlo, asu(a[2]), asu(b[0]))));
  const float r1 = asf(pick(mlo, asu(a[1]), asu(b[3]))) + dpp<0x128>(asf(pick(mlo, asu(a[3]), asu(b[1]))));
  return v2u{nz(asu(r0)), nz(asu(r1))};
}
__device__ __forceinline__ v2u join32(unsigned a) { auto t = __builtin_amdgcn_permlane32_swap(a, a, false, false); return v2u{t[0], t[1]}; }
__device__ __forceinline__ float qdiv(float a, float d, float inv) { const float q = a * inv; return fmaf(fmaf(-q, d, a), inv, q); }
__device__ __forceinline__ unsigned since(u64 t0) { return (unsigned)(__builtin_amdgcn_s_memrealtime() - t0); }
__device__ __forceinline__ void until(u64 t0, unsigned T) {
#pragma nounroll
  for (int it = 0; it < 4096; ++it) {
    if (since(t0) >= T) break;
    __builtin_amdgcn_s_sleep(1);
  }
}
__device__ __forceinline__ void setf(unsigned off) {
  asm volatile("s_mov_b64 exec, 1\n\tds_write_b8 %0, %1\n\ts_mov_b64 exec, -1" :: "v"(off), "v"(1u) : "memory");
}
__device__ __forceinline__ bool flag(LAS u8* L, int off) { return __builtin_amdgcn_readfirstlane(*(volatile LAS u8*)(L + off)) != 0; }
__device__ __forceinline__ void wait8(LAS u8* L, int off) {
#pragma nounroll
  for (unsigned it = 0; it < SPIN_L; ++it) {
    const v2u v = *(volatile LAS v2u*)(L + off);
    if (__builtin_amdgcn_readfirstlane(v[0] & v[1]) == 0x01010101) break;
  }
  CBAR();
}

__device__ __forceinline__ void wait8s(LAS u8* L, int off) {
#pragma nounroll
  for (unsigned it = 0; it < SPIN_L; ++it) {
    const v2u v = *(volatile LAS v2u*)(L + off);
    if (__builtin_amdgcn_readfirstlane(v[0] & v[1]) == 0x01010101) break;
    __builtin_amdgcn_s_sleep(1);
  }
  CBAR();
}
__device__ __forceinline__ bool all16(LAS u8* L, int off) {
  const v4u v = *(volatile LAS v4u*)(L + off);
  return __builtin_amdgcn_readfirstlane((v[0] & v[1]) & (v[2] & v[3])) == 0x01010101;
}
__device__ __forceinline__ void colsum(LAS u8* p, float& c0, float& c1) {
  const f32x4 t = ((ld4(p) + ld4(p + 2048)) + (ld4(p + 4096) + ld4(p + 6144))) + ((ld4(p + 8192) + ld4(p + 10240)) + (ld4(p + 12288) + ld4(p + 14336)));
  c0 = t32(t[0], t[2]); c1 = t32(t[1], t[3]);
}
__device__ __forceinline__ unsigned mx(v4u a) { return max(max(a[0], a[1]), max(a[2], a[3])); }
__device__ __forceinline__ unsigned mx(v2u a) { return max(a[0], a[1]); }
__device__ __forceinline__ u64 emp(unsigned m) { return __builtin_amdgcn_ballot_w64(m == 0xffffffffu); }
__device__ __forceinline__ bool any(bool p) { return __builtin_amdgcn_ballot_w64(p) != 0; }
#define BL4 "buffer_load_dwordx4 "
#define BL2 "buffer_load_dwordx2 "
#define EXM "s_mov_b64 exec, "
#define SWEND "s_mov_b64 exec, -1\n\ts_waitcnt vmcnt(0)"
#define SPIN(all, masks, sweep) if (__builtin_expect(emp(all) != 0, 0)) { _Pragma("nounroll") for (unsigned it_ = 0; it_ < SPIN_G; ++it_) { \
    masks; nap<1>(); sweep; if (emp(all) == 0) break; } }
#define WAIT0(...) asm volatile("s_waitcnt vmcnt(0)" : __VA_ARGS__ :: "memory")
__device__ __forceinline__ void ask1x4(v4u rsv, unsigned off, v4u& a) {
  asm volatile(BL4 "%0, %1, %2, 0 offen sc1" : "+v"(a) : "v"(off), "s"(rsv) : "memory");
}
__device__ __forceinline__ void got1x4(v4u rsv, unsigned off, v4u& a) {
  WAIT0("+v"(a));
  SPIN(mx(a), const u64 m0 = emp(mx(a)),
       asm volatile(EXM "%1\n\t" BL4 "%0, %2, %3, 0 offen sc1\n\t" SWEND : "+v"(a) : "s"(m0), "v"(off), "s"(rsv) : "memory"))
}
__device__ __forceinline__ void ask2x4(v4u rsv, unsigned off, v4u& a, v4u& b) {
  asm volatile(BL4 "%0, %2, %3, 0 offen sc1\n\t" BL4 "%1, %2, %3, 0 offen offset:1024 sc1" : "+v"(a), "+v"(b) : "v"(off), "s"(rsv) : "memory");
}
__device__ __forceinline__ void got2x4(v4u rsv, unsigned off, v4u& a, v4u& b) {
  WAIT0("+v"(a), "+v"(b));
  SPIN(max(mx(a), mx(b)), const u64 m0 = emp(mx(a)); const u64 m1 = emp(mx(b)),
       asm volatile(EXM "%2\n\t" BL4 "%0, %4, %5, 0 offen sc1\n\t" EXM "%3\n\t" BL4 "%1, %4, %5, 0 offen offset:1024 sc1\n\t" SWEND
                    : "+v"(a), "+v"(b) : "s"(m0), "s"(m1), "v"(off), "s"(rsv) : "memory"))
}
__device__ __forceinline__ void get4x4(v4u rsv, unsigned off, v4u (&a)[4]) {
  asm volatile(BL4 "%0, %4, %5, 0 offen sc1\n\t" BL4 "%1, %4, %5, 0 offen offset:1024 sc1\n\t" BL4 "%2, %4, %5, 0 offen offset:2048 sc1\n\t"
               BL4 "%3, %4, %5, 0 offen offset:3072 sc1\n\ts_waitcnt vmcnt(0)"
               : "=&v"(a[0]), "=&v"(a[1]), "=&v"(a[2]), "=&v"(a[3]) : "v"(off), "s"(rsv) : "memory");
  SPIN(max(max(mx(a[0]), mx(a[1])), max(mx(a[2]), mx(a[3]))),
       const u64 m0 = emp(mx(a[0])); const u64 m1 = emp(mx(a[1])); const u64 m2 = emp(mx(a[2])); const u64 m3 = emp(mx(a[3])),
       asm volatile(EXM "%4\n\t" BL4 "%0, %8, %9, 0 offen sc1\n\t" EXM "%5\n\t" BL4 "%1, %8, %9, 0 offen offset:1024 sc1\n\t"
                    EXM "%6\n\t" BL4 "%2, %8, %9, 0 offen offset:2048 sc1\n\t" EXM "%7\n\t" BL4 "%3, %8, %9, 0 offen offset:3072 sc1\n\t" SWEND
                    : "+v"(a[0]), "+v"(a[1]), "+v"(a[2]), "+v"(a[3]) : "s"(m0), "s"(m1), "s"(m2), "s"(m3), "v"(off), "s"(rsv) : "memory"))
}
__device__ __forceinline__ void ask2x2(v4u rsv, unsigned o0, unsigned o1, v2u& a, v2u& b) {
  asm volatile(BL2 "%0, %2, %4, 0 offen sc1\n\t" BL2 "%1, %3, %4, 0 offen sc1" : "+v"(a), "+v"(b) : "v"(o0), "v"(o1), "s"(rsv) : "memory");
}
__device__ __forceinline__ void got2x2(v4u rsv, unsigned o0, unsigned o1, v2u& a, v2u& b) {
  WAIT0("+v"(a), "+v"(b));
  SPIN(max(mx(a), mx(b)), const u64 m0 = emp(mx(a)); const u64 m1 = emp(mx(b)),
       asm volatile(EXM "%2\n\t" BL2 "%0, %4, %6, 0 offen sc1\n\t" EXM "%3\n\t" BL2 "%1, %5, %6, 0 offen sc1\n\t" SWEND
                    : "+v"(a), "+v"(b) : "s"(m0), "s"(m1), "v"(o0), "v"(o1), "s"(rsv) : "memory"))
}
#define L4P(e0, e1) e0 BL2 "%2, %4, %6, 0 offen sc1\n\t" BL4 "%0, %5, %6, 0 offen sc1\n\t" \
    e1 BL2 "%3, %4, %6, 0 offen offset:512 sc1\n\t" BL4 "%1, %5, %6, 0 offen offset:1024 sc1\n\t"
#define O4P "+v"(a[0]), "+v"(a[1]), "+v"(z[0]), "+v"(z[1])
__device__ __forceinline__ void ask4p(v4u rsv, unsigned om, unsigned off, v2u (&z)[2], v4u (&a)[2]) {
  asm volatile(L4P("", "") : O4P : "v"(om), "v"(off), "s"(rsv) : "memory");
}
__device__ __forceinline__ void got4p(v4u rsv, unsigned om, unsigned off, v2u (&z)[2], v4u (&a)[2]) {
  WAIT0(O4P);
  SPIN(max(max(mx(a[0]), mx(z[0])), max(mx(a[1]), mx(z[1]))),
       const u64 m0 = emp(max(mx(a[0]), mx(z[0]))); const u64 m1 = emp(max(mx(a[1]), mx(z[1]))),
       asm volatile(L4P(EXM "%7\n\t", EXM "%8\n\t") SWEND : O4P : "v"(om), "v"(off), "s"(rsv), "s"(m0), "s"(m1) : "memory"))
}
#define FLAGS_WAIT(F, sl) _Pragma("nounroll") for (unsigned it_ = 0; it_ < SPIN_G; ++it_) { \
    const u64 fa_ = ALD64(F + lane * 8), fb_ = ALD64(F + 512 + lane * 8); \
    if (!any(((fa_ ^ tag) | (fb_ ^ tag)) != 0)) break; __builtin_amdgcn_s_sleep(sl); }

#define TAG() (*(const volatile __attribute__((address_space(4))) u64*)(ws + WS_EPOCH) + 1)

__global__ void __launch_bounds__(1024, 4)
fused_decode_kernel(const u8* __restrict__ wq, const u8* __restrict__ x, u8* ws, const u8* __restrict__ kc,
                    const u8* __restrict__ vc, const u8* __restrict__ wo, const u8* __restrict__ wg, const u8* __restrict__ wu,
                    const u8* __restrict__ wd, u8* __restrict__ out) {
  extern __shared__ __attribute__((aligned(16))) u8 smem[];
  LAS u8* L = (LAS u8*)smem;
  const u64 t0 = __builtin_amdgcn_s_memrealtime();
  u64 pc0;
  asm volatile("s_getpc_b64 %0" : "=s"(pc0));
  const int c = blockIdx.x, xcd = c & 7, i16 = (c >> 3) & 15, u = xcd * 16 + i16;
  const bool isx = c < 128;
  const int tid = threadIdx.x, lane = tid & 63, pw_ = __builtin_amdgcn_readfirstlane(tid >> 6);
  const int w = isx ? (pw_ & 3) + ((0x1302 >> (pw_ & 12)) & 3) * 4 : pw_ ^ 8;
  const int b7 = lane & 7, h3 = (lane >> 3) & 1, kg = lane >> 4, c15 = lane & 15, hi = lane >> 5;
  const int hq = i16 * 2 + (xcd >> 2), ksq = xcd & 3;
  const v4u rsv = {(unsigned)(u64)ws, (unsigned)((u64)ws >> 32), WS_END, 0x00020000u};
  v4u xa0, xa1, R[8];
  {
    unsigned sel = (c >> 7) * 4 + (w >> 2), so = (256 * ksq + 32 * (w & 7)) * 8192 + hq * 256, xo = (w & 7) * 2048;
    unsigned lo = ((unsigned)kg << 16) + c15 * 16, l16 = lane * 16;
    asm volatile("" : "+s"(sel), "+s"(so), "+s"(xo), "+v"(lo), "+v"(l16));
    const v4u rw = {(unsigned)(u64)wq, (unsigned)((u64)wq >> 32), 8u << 20, 0x00020000u};
    const v4u rx = {(unsigned)(u64)x, (unsigned)((u64)x >> 32), 16384u, 0x00020000u};
#define L1_(n) "s_add_u32 %10, %10, 0x2000\n\tbuffer_load_dwordx4 %" #n ", %12, %13, %10 offen nt\n\t"
    asm volatile("s_cmp_lt_u32 %11, 2\n\ts_cbranch_scc1 .Lq%=\n\ts_cmp_gt_u32 %11, 3\n\ts_cbranch_scc1 .Lskip%=\n\t"
                 "buffer_load_dwordx4 %8, %14, %15, %16 offen\n\tbuffer_load_dwordx4 %9, %14, %15, %16 offen offset:1024\n\t"
                 "s_branch .Lskip%=\n\t"
                 ".Lq%=:\n\tbuffer_load_dwordx4 %0, %12, %13, %10 offen nt\n\t"
                 L1_(1) L1_(2) L1_(3) L1_(4) L1_(5) L1_(6) L1_(7) ".Lskip%=:"
                 : "=&v"(R[0]), "=&v"(R[1]), "=&v"(R[2]), "=&v"(R[3]), "=&v"(R[4]), "=&v"(R[5]), "=&v"(R[6]), "=&v"(R[7]),
                   "=&v"(xa0), "=&v"(xa1), "+s"(so)
                 : "s"(sel), "v"(lo), "s"(rw), "v"(l16), "s"(rx), "s"(xo), "s"(t0) : "memory", "scc");
  }
  unsigned pf = 0;
  if (!isx && w < 3) {
    const unsigned ln = min(tid - 8 * 64, CODE_LINES - 1);
    asm volatile("global_load_dword %0, %1, %2" : "=v"(pf) : "v"(ln * 128), "s"(pc0 & ~127ull) : "memory");
  }
  const __amdgpu_buffer_rsrc_t rs = __builtin_amdgcn_make_buffer_rsrc((void*)ws, 0, (int)WS_END, 0x00020000);
  if (!isx && w >= 8) {
    const unsigned slot = (u >> 4) * 128 + (((u >> 2) & 3) * 8 + (w & 7)) * 4 + (u & 3);
    ST64((v2u{0xffffffffu, 0xffffffffu}), QP + slot * 512 + lane * 8);
    const u64 tag = TAG();
    VM0();
    FLAG(WS_F1 + slot * 8);
  }
  unsigned mlo = h3 - 1u, mhi = ~mlo;
  const unsigned ecol = L_XN + h3 * 512 + kg * 128 + ((((b7 >> 1) + 4 * (b7 & 1)) ^ (4 * h3 + kg)) * 16);
  const f32x4 zero4 = {0.f, 0.f, 0.f, 0.f};
  const v4u empty4 = {0xffffffffu, 0xffffffffu, 0xffffffffu, 0xffffffffu};
  const v2u empty2 = {0xffffffffu, 0xffffffffu};
  const unsigned kvoff = ((u >> 2) * 4096 + 3072 + (u & 3) * 256 + (w & 7) * 32) * 256, kvl = lane * 16;
  constexpr unsigned KVS = 1024, KVB = 8192;
#define ROLE(id) int role##id = (c >> 7) * 2 + (w >> 3); asm volatile("" : "+s"(role##id)); if (role##id == id)
#define FORGET() asm volatile("" : "=v"(R[0]), "=v"(R[1]), "=v"(R[2]), "=v"(R[3]), "=v"(R[4]), "=v"(R[5]), "=v"(R[6]), "=v"(R[7]), "=v"(xa0), "=v"(xa1), "=v"(pf))
#define END_ROLE() do { asm volatile("s_endpgm" ::: "memory"); FORGET(); } while (0)
#define LANDED(n, ...) asm volatile("s_waitcnt vmcnt(" #n ")" : __VA_ARGS__ :: "memory")

  {
    ROLE(0) {
      v4u F[8];
      u64 t1;
      {
        unsigned qoff = QP + w * 65536 + (hq * 4 + ksq) * 512 + lane * 8;
        unsigned xa = L_XN + w * 512 + kg * 128 + ((b7 ^ ((w & 1) * 4 + kg)) * 16);
        unsigned wa = L_R0 + w * 4096 + b7 * 512 + (((kg * 2 + h3) ^ b7) * 16);
        unsigned ra = L_R0 + hi * 16384 + w * 512 + (lane & 3) * 128 + ((((lane >> 2) & 7) ^ w) * 16);
        PIN(qoff); PIN(xa); PIN(wa); PIN(ra); PIN(mlo); PIN(mhi);
        BAR();
        t1 = __builtin_amdgcn_s_memrealtime();
        const v4u x0 = *(LAS v4u*)(L + xa);
        v4u bl = x0 & mlo, bh = x0 & mhi;
        PIN(bl); PIN(bh);
        LANDED(2, "+v"(R[0]), "+v"(R[1]), "+v"(R[2]), "+v"(R[3]), "+v"(R[4]), "+v"(R[5]));
        trp<0, 3>(R, F);
        PIN8(F);
        LANDED(0, "+v"(R[6]), "+v"(R[7]));
        trp<3, 4>(R, F);
        tile_mma(L + wa, 128, F, bl, bh);
        BAR();
        const f32x2 v = fold8(L + ra);
        ST64((v2u{nz(asu(v[0])), nz(asu(v[1]))}), qoff);
      }
      BAR();
      const int rbk = u >> 3, cbd = u & 7;
      unsigned doff = DP + (cbd * 16 + kg * 4 + (w & 3)) * 4096 + rbk * 256 + (w >> 2) * 128 + c15 * 8;
      unsigned e0 = ecol;
      unsigned tr = L_R0 + w * 8192 + lane * 16;
      PIN(doff); PIN(e0); PIN(tr);
      if (w < 7) {
        const int hb = 7 * rbk + w, cb = hb >> 1;
        const __amdgpu_buffer_rsrc_t rw = TILE(wd + (size_t)(32 * hb) * 2048 + cbd * 256, 31 * 2048 + 256);
        unsigned lo = (unsigned)(4 * (kg >> 1) + (kg & 1)) * 2048u + c15 * 16;
        const bool gs = ((((unsigned)(u64)wg >> 7) + cb) & 7) == 3, us = ((((unsigned)(u64)wu >> 7) + cb) & 7) == 3;
        unsigned o0 = GP + hb * 512 + lane * 8, o1 = o0 + 57344;
        unsigned o2 = GP + (112 + (cb >> 3)) * 1024 + (hb & 1) * 512 + lane * 8, o3 = keep(us, o2 + 7168);
        o2 = keep(gs, o2);
        unsigned ew = L_XN + w * 512 + kg * 128 + ((b7 ^ ((w & 1) * 4 + kg)) * 16) + h3 * 8;
        v2u gu[4] = {{0u, 0u}, {0u, 0u}, {0u, 0u}, {0u, 0u}};
        PIN(lo); PIN(o0); PIN(o1); PIN(o2); PIN(o3); PIN(ew); PIN(gu[0]); PIN(gu[1]); PIN(gu[2]); PIN(gu[3]);
        BAR(); BAR(); BAR();
        until(__builtin_amdgcn_s_memrealtime(), w < 3 ? w * T_DNS : T_DN1 + (w - 3) * T_DNS);
#pragma unroll
        for (int j = 0; j < 8; ++j) R[j] = LDW(rw, lo, (8 * (j & 3) + 2 * (j >> 2)) * 2048);
        SB();
        tr8(R, F);
#pragma unroll
        for (int i = 0; i < 8; ++i) *(LAS v4u*)(L + L_R0 + i * 8192 + w * 1024 + lane * 16) = F[i];
        BAR();
        ask2x2(rsv, o0, o1, gu[0], gu[1]);
#pragma unroll
        for (int k = 0; k < 7; ++k) F[k] = *(LAS v4u*)(L + tr + k * 1024);
        PIN8(F);
        if (gs || us) { ask2x2(rsv, o2, o3, gu[2], gu[3]); got2x2(rsv, o2, o3, gu[2], gu[3]); }
        got2x2(rsv, o0, o1, gu[0], gu[1]);
        if (gs || us) {
#pragma unroll
          for (int d = 0; d < 2; ++d) {
            gu[0][d] = pk(lo16(gu[0][d]) + lo16(gu[2][d]), hi16(gu[0][d]) + hi16(gu[2][d]));
            gu[1][d] = pk(lo16(gu[1][d]) + lo16(gu[3][d]), hi16(gu[1][d]) + hi16(gu[3][d]));
          }
        }
        unsigned hv[2];
#pragma unroll
        for (int d = 0; d < 2; ++d) {
          const unsigned sg = pk(rcp(1.f + ex2(lo16(gu[0][d]) * -LOG2E)), rcp(1.f + ex2(hi16(gu[0][d]) * -LOG2E)));
          hv[d] = pk(lo16(sg) * lo16(gu[1][d]), hi16(sg) * hi16(gu[1][d]));
        }
        *(LAS v2u*)(L + ew) = v2u{hv[0], hv[1]};
        BAR();
        ST64(col_mma<7>(L + e0, F, mlo), doff);
      } else {
        PRIO(3);
        const int bb = (lane & 3) + 4 * hi;
        const int rcol = 128 * (u >> 4) + 32 * ((u >> 2) & 3) + (u & 3) + 4 * h3 + 16 * ((lane >> 2) & 1);
        const u64 tag = TAG();
        unsigned poff = WP + u * 4096 + lane * 16;
        unsigned hoff = keep((lane & 28) == 0, HF + ((u >> 3) * 8 + (u & 3) * 2 + ((u >> 2) & 1)) * 256 + bb * 16);
        unsigned ooff = (bb * 1024 + rcol + 8 * (kg & 1)) * 2;
        v4u pr[4];
        float x0 = lo16(*(const GAS unsigned short*)(x + (bb * 1024 + rcol) * 2)), x1 = lo16(*(const GAS unsigned short*)(x + (bb * 1024 + rcol + 8) * 2));
        PIN(x0); PIN(x1); PIN(poff); PIN(hoff); PIN(ooff);
        until(t1, T_F3);
        FLAGS_WAIT(WS_F2, 8)
        BAR(); BAR();
        nap<D_H3>();
        get4x4(rsv, poff, pr);
        unsigned hp;
        {
          const f32x4 s = (__builtin_bit_cast(f32x4, pr[0]) + __builtin_bit_cast(f32x4, pr[1])) + (__builtin_bit_cast(f32x4, pr[2]) + __builtin_bit_cast(f32x4, pr[3]));
          const float a = t32(s[0], s[2]), b = t32(s[1], s[3]);
          const unsigned at = pk(t16(a, a), t16(b, b));
          hp = pk(x0 + lo16(at), x1 + hi16(at));
          const float hz = asf(nz(hp));
          const v4u h4 = {asu(hz), asu(dpp<0x104>(hz)), asu(dpp<0x108>(hz)), asu(dpp<0x10C>(hz))};
          asm volatile("s_mov_b64 exec, %3\n\tbuffer_store_dwordx4 %0, %1, %2, 0 offen sc1\n\ts_mov_b64 exec, -1\n\ts_nop 0"
                       :: "v"(h4), "v"(hoff), "s"(rsv), "s"(0x0000000f0000000full) : "memory");
        }
        BAR();
        float hs = hi16(hp << (16 - (kg & 1) * 16));
        poff += DP - WP;
        PIN(hs); PIN(poff);
        BAR();
#pragma unroll
        for (int k = 0; k < 7; ++k) F[k] = *(LAS v4u*)(L + tr + k * 1024);
        PIN8(F);
        BAR();
        ST64(col_mma<7>(L + e0, F, mlo), doff);
        nap<D_H6>();
        get4x4(rsv, poff, pr);
        {
          const f32x4 s = (__builtin_bit_cast(f32x4, pr[0]) + __builtin_bit_cast(f32x4, pr[1])) + (__builtin_bit_cast(f32x4, pr[2]) + __builtin_bit_cast(f32x4, pr[3]));
          const __bf16 res = (__bf16)(hs + rb(t16(t32(s[0], s[2]), t32(s[1], s[3]))));
          __builtin_amdgcn_raw_buffer_store_b16(__builtin_bit_cast(unsigned short, res), TILE(out, 16384), (int)ooff, 0, 0);
        }
        if (c == 0) FLAG(WS_EPOCH);
      }
      END_ROLE();
    }
    ROLE(1) {
      const int v = w - 8, hp = u >> 3, cbo = u & 7, ho = 2 * hp + (v >> 2), pw = v & 3;
      const u64 tag = TAG();
      PRIO(3);
      LANDED(0, "+v"(xa0), "+v"(xa1));
      {
        const float pv = asf(__builtin_amdgcn_readfirstlane(xa0[0]) << 16);
        float s1[4], s2[4];
#pragma unroll
        for (int m = 0; m < 4; ++m) {
          const float a = lo16(xa0[m]) - pv, b = hi16(xa0[m]) - pv, d = lo16(xa1[m]) - pv, e = hi16(xa1[m]) - pv;
          s1[m] = (a + b) + (d + e);
          s2[m] = fmaf(a, a, b * b) + fmaf(d, d, e * e);
        }
        const float ra = row_sum((s1[0] + s1[1]) + (s1[2] + s1[3])), rq = row_sum((s2[0] + s2[1]) + (s2[2] + s2[3]));
        const float tt = t16(ra, rq), t2 = t32(tt, tt);
        const float dm = asf(__builtin_amdgcn_readlane(asu(t2), 0)) * (1.f / 1024.f);
        const float var = fmaxf(asf(__builtin_amdgcn_readlane(asu(t2), 16)) * (1.f / 1024.f) - dm * dm, 0.f);
        const float mb = rb(pv + dm), den = rb(__builtin_amdgcn_sqrtf(rb(rb(var) + 1e-5f)));
        const float inv = rcp(den);
        const unsigned hsel = ksq & 2 ? 0xffffffffu : 0u;
        v4u xn;
#pragma unroll
        for (int m = 0; m < 4; ++m) {
          const unsigned xs = (xa0[m] & ~hsel) | (xa1[m] & hsel);
          const unsigned dd = pk(lo16(xs) - mb, hi16(xs) - mb);
          xn[m] = pk(qdiv(lo16(dd), den, inv), qdiv(hi16(dd), den, inv));
        }
        *(LAS v4u*)(L + L_XN + (lane ^ (ksq & 1) * 32) * 128 + ((v ^ b7) * 16)) = xn;
      }
      const unsigned foff = WS_F1 + ((lane >> 3) * 128 + hq * 4 + ksq) * 8;
      v2u fv = empty2;
      if (v == 0) {
        until(t0, T_FX);
        asm volatile(BL2 "%0, %1, %2, 0 offen sc1" : "+v"(fv) : "v"(foff), "s"(rsv) : "memory");
      }
      BAR();
      const u64 t1 = __builtin_amdgcn_s_memrealtime();
      const __amdgpu_buffer_rsrc_t rw = TILE(wo + (size_t)(128 * ho + 16 * (pw >> 1) + 2 * (pw & 1)) * 2048 + cbo * 256, 109 * 2048 + 256);
      unsigned lo = (unsigned)(64 * (kg & 1) + (kg >> 1)) * 2048u + c15 * 16;
      unsigned moff = ML + (v * 4 + (hp >> 2)) * 1024 + hi * 256 + ((hp & 3) * 2 + (kg & 1)) * 8;
      unsigned ooff = OP + (hp * 8 + v) * 2048 + lane * 16;
      unsigned ew = L_XN + (lane & 31) * 128 + ((v ^ b7) * 16) + hi * 8;
      unsigned e0 = ecol;
      unsigned poff = WP + (cbo * 16 + kg * 4 + pw) * 4096 + hp * 256 + (v >> 2) * 128 + c15 * 8;
      v2u ml[2] = {empty2, empty2};
      v4u o[2] = {empty4, empty4};
      PIN(lo); PIN(moff); PIN(ooff); PIN(poff); PIN(ew); PIN(e0); PIN(mlo);
      PIN(ml[0]); PIN(ml[1]); PIN(o[0]); PIN(o[1]);
      if (v == 0) {
        WAIT0("+v"(fv));
        if (any(((fv[0] ^ (unsigned)tag) | (fv[1] ^ (unsigned)(tag >> 32))) != 0)) {
#pragma nounroll
          for (unsigned it = 0; it < SPIN_G; ++it)
            if (!any(ALD64(foff) != tag)) break;
        }
      }
      BAR();
      PRIO(0);
      BAR();
      until(t1, v < WO_N ? T_WO + v * T_WOS : T_WO2 + (v - WO_N) * T_WOS2);
      v4u F[8];
#pragma unroll
      for (int j = 0; j < 8; ++j) R[j] = LDW(rw, lo, (32 * (j >> 2) + 8 * (j & 1) + 4 * ((j >> 1) & 1)) * 2048);
      SB();
      tr8(R, F);
#pragma unroll
      for (int i = 0; i < 8; ++i) *(LAS v4u*)(L + L_R0 + i * 8192 + v * 1024 + lane * 16) = F[i];
      BAR();
      ask4p(rsv, moff, ooff, ml, o);
#pragma unroll
      for (int k = 0; k < 8; ++k) F[k] = *(LAS v4u*)(L + L_R0 + v * 8192 + k * 1024 + lane * 16);
      PIN8(F);
      got4p(rsv, moff, ooff, ml, o);
      {
        const float mh = fmaxf(asf(ml[0][0]), asf(ml[1][0]));
        auto ms = __builtin_amdgcn_permlane32_swap(asu(mh), asu(mh), false, false);
        const float M = fmaxf(asf(ms[0]), asf(ms[1]));
        const float w0 = ex2((asf(ml[0][0]) - M) * LOG2E), w1 = ex2((asf(ml[1][0]) - M) * LOG2E);
        const float lh = fmaf(w0, asf(ml[0][1]), w1 * asf(ml[1][1]));
        const float inv = rcp(t32(lh, lh));
        float e[8];
#pragma unroll
        for (int d = 0; d < 4; ++d) { e[2 * d] = fmaf(w0, lo16(o[0][d]), w1 * lo16(o[1][d])); e[2 * d + 1] = fmaf(w0, hi16(o[0][d]), w1 * hi16(o[1][d])); }
        unsigned hv[2];
#pragma unroll
        for (int d = 0; d < 2; ++d) hv[d] = pk(t32(e[2 * d], e[4 + 2 * d]) * inv, t32(e[2 * d + 1], e[5 + 2 * d]) * inv);
        *(LAS v2u*)(L + ew) = v2u{hv[0], hv[1]};
      }
      BAR();
      ST64(col_mma<8>(L + e0, F, mlo), poff);
      END_ROLE();
    }
  }
  {
    const int bg = u >> 2, sp = u & 3, a = w & 7;
    int mat, cb, unit, half = -1;
    if (i16 < 14) {
      unit = xcd * 14 + i16; mat = unit >= 56; cb = unit - mat * 56;
      if (((((unsigned)(u64)(mat ? wu : wg) >> 7) + cb) & 7) == 3) half = 0;
    } else {
      const int k = xcd * 2 + i16 - 14;
      unit = 112 + k; mat = k >= 7; half = 1;
      cb = ((3 - ((unsigned)(u64)(mat ? wu : wg) >> 7)) & 7) + 8 * (k - 7 * mat);
    }
    const bool mlp = unit < 126;
    const u64 tag = TAG();
    ROLE(2) {
      if (a == 7) {
        const unsigned oo = OP + (((bg & 3) * 4 + hi) * 8 + (bg >> 2)) * 2048 + sp * 512 + (lane & 31) * 16;
        ST128(empty4, oo);
        ST128(empty4, oo + 2 * 16384);
        ST128(empty4, GP + unit * 1024 + lane * 16);
        ST128(empty4, keep(lane < 4, ML + u * 256 + lane * 16));
        ST128(empty4, keep(lane < 8, HF + u * 256 + lane * 16));
#pragma unroll
        for (int k = 0; k < 8; ++k) ST128(empty4, WP + (k >> 2) * (DP - WP) + u * 4096 + (k & 3) * 1024 + lane * 16);
      }
      VM0();
      asm volatile("" :: "v"(pf));
      if (a == 7) FLAG(WS_F2 + u * 8);
      const __amdgpu_buffer_rsrc_t rw = TILE(vc + kvoff, KVB);
      unsigned wa = L_R0 + a * 4096 + b7 * 512 + (((kg * 2 + h3) ^ b7) * 16);
      unsigned ra = L_R0 + hi * 16384 + a * 512 + ((lane >> 3) & 3) * 128 + ((b7 ^ a) * 16);
      unsigned ooff = OP + (((bg & 3) * 4 + (a >> 1)) * 8 + (bg >> 2)) * 2048 + sp * 512 + (a & 1) * 256 + lane * 4;
      unsigned pw = L_P + a * 512 + kg * 128 + b7 * 16;
      PIN(wa); PIN(ra); PIN(ooff); PIN(pw); PIN(mlo); PIN(mhi);
      BAR();
      BAR();
#pragma unroll
      for (int j = 0; j < 8; ++j) {
        if (j == VEARLY) {
          wait8s(L, L_FL);
        }
        R[j] = LDW(rw, kvl, j * KVS);
        SB();
      }
      PRIO(3);
      BAR();
      {
        const v4u P = *(LAS v4u*)(L + pw);
        v4u pl = P & mlo, ph = P & mhi;
        PIN(pl); PIN(ph);
        v4u F[8];
        tr8(R, F);
        tile_mma(L + wa, 128, F, pl, ph);
      }
      BAR();
      const f32x2 t = fold8(L + ra);
      ST32(nz(pk(t[0], t[1])), ooff);
      PRIO(0);
      FORGET();
    }
    ROLE(3) {
      PRIO(3);
      if (a == 0) { *(volatile LAS unsigned*)(L + L_FL + lane * 4) = 0u; *(volatile LAS unsigned*)(L + L_MX + lane * 4) = 0xff800000u; }
      unsigned qoff = QP + (bg >> 2) * 65536 + ((bg & 3) * 8 + a) * 2048 + lane * 16;
      unsigned qw = L_QF + ((lane >> 2) & 3) * 512 + ((kg & 1) * 2 + hi) * 128 + a * 16 + (lane & 3) * 4;
      unsigned qa = L_QF + kg * 128 + b7 * 16, ma = L_MX + b7 * 4, pw = L_P + a * 512 + kg * 128 + b7 * 16 + h3 * 8;
      unsigned f1 = L_FL + 8 + a, f2 = L_FL + 16 + a;
      const unsigned foff = WS_F1 + ((bg >> 2) * 128 + (bg & 3) * 32 + (lane & 31)) * 8;
      v4u p0 = empty4, p1 = empty4;
      unsigned kw[4];
#pragma unroll
      for (int j = 0; j < 4; ++j) { kw[j] = L_R1 + a * 8192 + kg * 256 + ((c15 ^ (4 * j + kg)) * 16); PIN(kw[j]); }
      PIN(qoff); PIN(qw); PIN(qa); PIN(ma); PIN(pw); PIN(f1); PIN(f2); PIN(p0); PIN(p1);
      bool f1ok = false;
      if (a == 7) {
        until(t0, T_F1);
#pragma nounroll
        for (unsigned it = 0; it < 64; ++it) {
          f1ok = !any(ALD64(foff) != tag);
          if (f1ok || since(t0) >= T_K1) break;
        }
      }
      if (a >= KE) BAR();
      else until(t0, T_F1 + T_KE);
      u64 t1 = __builtin_amdgcn_s_memrealtime();
      {
        const u64 base = (u64)kc + kvoff;
        const v4u rw = {(unsigned)base, (unsigned)(base >> 32) & 0xffffu, KVB, 0x00020000u};
        unsigned so = 0;
#define LK_(n) "buffer_load_dwordx4 %" #n ", %9, %10, %8 offen nt\n\ts_add_u32 %8, %8, %11\n\t"
        asm volatile(LK_(0) LK_(1) LK_(2) LK_(3) LK_(4) LK_(5) LK_(6) LK_(7)
                     : "=&v"(R[0]), "=&v"(R[1]), "=&v"(R[2]), "=&v"(R[3]), "=&v"(R[4]), "=&v"(R[5]), "=&v"(R[6]), "=&v"(R[7]), "+s"(so)
                     : "v"(kvl), "s"(rw), "i"(KVS) : "memory", "scc");
      }
      if (a < KE) { BAR(); t1 = __builtin_amdgcn_s_memrealtime(); }
      BAR();
      if (a == 7) {
#pragma nounroll
        for (unsigned it = 0; it < SPIN_G && !f1ok; ++it) f1ok = !any(ALD64(foff) != tag);
        setf(L_FL + 56);
      }
#pragma nounroll
      for (unsigned it = 0; it < SPIN_L && !flag(L, L_FL + 56); ++it) __builtin_amdgcn_s_sleep(1);
      until(t1, T_Q);
      v4u f2v = empty4;
      ask2x4(rsv, qoff, p0, p1);
      setf(L_FL + a);
      vmw<2>();
      v4u K[8];
      {
        LAS u8* kb = L + L_R1 + a * 8192;
        const int krow = (lane & 3) * 4 + (c15 >> 2);
#pragma unroll
        for (int j = 0; j < 8; ++j)
          asm volatile("ds_write_b128 %0, %1 offset:%2" :: "v"(kw[j & 3]), "v"(R[j]), "i"((j >> 2) * 4096 + (j & 3) * 1024) : "memory");
#pragma unroll
        for (int j = 0; j < 8; ++j) K[j] = *(LAS v4u*)(kb + (j >> 2) * 4096 + krow * 256 + (((4 * (j & 3) + kg) ^ krow) * 16));
        PIN8(K);
      }
      got2x4(rsv, qoff, p0, p1);
      {
        const f32x4 q = __builtin_bit_cast(f32x4, p0) + __builtin_bit_cast(f32x4, p1);
        *(LAS unsigned*)(L + qw) = pk(t32(q[0], q[1]), t32(q[2], q[3]));
      }
      setf(f1);
      if (a == 1) ask1x4(rsv, WS_F2 + lane * 16, f2v);
      wait8(L, L_FL + 8);
      v4u qf[4];
#pragma unroll
      for (int j = 0; j < 4; ++j) qf[j] = *(LAS v4u*)(L + qa + j * 512);
      SB();
      f32x4 s0 = zero4, s1 = zero4;
#pragma unroll
      for (int j = 0; j < 4; ++j) { s0 = mfma(K[j], qf[j], s0); s1 = mfma(K[4 + j], qf[j], s1); }
      float sc[4];
      {
        const unsigned ta = pick(mlo, pk(s0[0], s0[1]), pk(s1[0], s1[1])), tb = pick(mlo, pk(s0[2], s0[3]), pk(s1[2], s1[3]));
        const unsigned ua = pk(lo16(ta) * 0.08838834764831845f, hi16(ta) * 0.08838834764831845f);
        const unsigned ub = pk(lo16(tb) * 0.08838834764831845f, hi16(tb) * 0.08838834764831845f);
        sc[0] = lo16(ua); sc[1] = hi16(ua); sc[2] = lo16(ub); sc[3] = hi16(ub);
      }
      asm volatile("ds_max_f32 %0, %1" :: "v"(ma), "v"(fmaxf(fmaxf(sc[0], sc[1]), fmaxf(sc[2], sc[3]))) : "memory");
      setf(f2);
      wait8(L, L_FL + 16);
      const float M = *(volatile LAS float*)(L + ma);
#pragma unroll
      for (int r = 0; r < 4; ++r) sc[r] = ex2((sc[r] - M) * LOG2E);
      *(LAS v2u*)(L + pw) = v2u{pk(sc[0], sc[1]), pk(sc[2], sc[3])};
      BAR();
      float ls = (sc[0] + sc[1]) + (sc[2] + sc[3]);
      ls += dpp<0x128>(ls);
      ls = t16(ls, ls);
      ls = t32(ls, ls);
      *(LAS float*)(L + L_STA + b7 * 32 + a * 4) = ls;
      setf(L_FL + 24 + a);
      if (a == 0) {
        wait8(L, L_FL + 24);
        const f32x4 l4 = ld4(L + L_STA + b7 * 32) + ld4(L + L_STA + b7 * 32 + 16);
        ST64((v2u{nz(asu(M)), nz(asu((l4[0] + l4[1]) + (l4[2] + l4[3])))}), keep(lane < 8, ML + u * 256 + lane * 8));
      }
      PRIO(0);
      if (!(mlp && a >= 8 - GUE)) BAR();
      WAIT0("+v"(f2v));
      if (a == 1 && !any((((f2v[0] ^ (unsigned)tag) | (f2v[1] ^ (unsigned)(tag >> 32))) | ((f2v[2] ^ (unsigned)tag) | (f2v[3] ^ (unsigned)(tag >> 32)))) != 0)) setf(L_FL + 48);
    }
    if (mlp) {
      const __amdgpu_buffer_rsrc_t rw = TILE((mat ? wu : wg) + (size_t)(64 * w) * 7168 + cb * 128, 63 * 7168 + 128);
      unsigned lo = (unsigned)(h3 * 32 + kg) * 7168u + b7 * 16;
      unsigned hoff = HF + (w * 8 + (lane >> 3)) * 256 + b7 * 16;
      PIN(lo); PIN(hoff);
      const bool early = w >= 16 - GUE;
      if (!early) BAR();
      u64 t2 = __builtin_amdgcn_s_memrealtime();
      if (w == 9 && !flag(L, L_FL + 48)) { FLAGS_WAIT(WS_F2, 4) setf(L_FL + 48); }
      v4u F[8], hf = empty4;
      if (half < 0 || half == (w >> 3)) {
#pragma unroll
        for (int j = 0; j < 8; ++j) R[j] = LDW(rw, lo, (8 * (j & 3) + 4 * (j >> 2)) * 7168);
        SB();
        if (early) { BAR(); BAR(); t2 = __builtin_amdgcn_s_memrealtime(); }
        if (since(t2) >= T_H - 40 && flag(L, L_FL + 48)) hf = __builtin_amdgcn_raw_buffer_load_b128(rs, (int)hoff, 0, 16);
        SB();
        v2u X[16];
#pragma unroll
        for (int j = 0; j < 8; ++j)
#pragma unroll
          for (int d = 0; d < 2; ++d) {
            X[j][d] = (unsigned)__builtin_amdgcn_update_dpp((int)R[j][d], (int)R[j][d + 2], 0x128, 0xf, 0xC, false);
            X[8 + j][d] = (unsigned)__builtin_amdgcn_update_dpp((int)R[j][d + 2], (int)R[j][d], 0x128, 0xf, 0x3, false);
          }
        tr4(X, F);
      } else {
        if (early) { BAR(); BAR(); t2 = __builtin_amdgcn_s_memrealtime(); }
#pragma unroll
        for (int j = 0; j < 8; ++j) F[j] = v4u{0u, 0u, 0u, 0u};
      }
      PIN8(F);
      v4u ones = {0x3f803f80u, 0x3f803f80u, 0x3f803f80u, 0x3f803f80u};
      unsigned wa = L_R1 + w * 2048 + lane * 16, sw = kg == (c15 >> 2) ? L_STG + b7 * 272 + w * 16 + h3 * 8 : L_QF + lane * 8;
      unsigned m1 = 0u - (lane & 1), m2 = 0u - ((lane >> 1) & 1);
      unsigned ra = L_R1 + hi * 16384 + (kg & 1) * 1024 + ((w & 3) * 16 + c15) * 16, sr = L_STG + b7 * 272 + kg * 64 + h3 * 32;
      unsigned goff = GP + unit * 1024 + (w & 1) * 512 + ((w >> 1) & 1) * 256 + (lane & 31) * 8;
      const bool fin = (w >> 2) == 2;
      {
        const v4u ol = ones & mlo, oh = ones & mhi;
#pragma unroll
        for (int i = 0; i < 2; ++i)
          *(LAS f32x4*)(L + wa + i * 1024) = mfma(F[4 + i], ol, mfma(F[2 + i], oh, mfma(F[6 + i], oh, mfma(F[i], ol, zero4))));
      }
      setf(L_FL + 64 + w);
      PIN(goff); PIN(wa); PIN(sw); PIN(ra); PIN(sr); PIN(m1); PIN(m2); PIN(ones); PIN(hf);
      float cs0 = 0.f, cs1 = 0.f;
      bool havecs = false;
      if (emp(mx(hf))) {
        bool tried = false;
#pragma nounroll
        for (unsigned it = 0; it < SPIN_L; ++it) {
          if (all16(L, L_FL + 64) && flag(L, L_FL + 48)) break;
          if (!tried && since(t2) >= T_H && flag(L, L_FL + 48)) { tried = true; ask1x4(rsv, hoff, hf); WAIT0("+v"(hf)); if (!emp(mx(hf))) break; }
          __builtin_amdgcn_s_sleep(1);
        }
        if (emp(mx(hf))) {
          ask1x4(rsv, hoff, hf);
          if (fin) { colsum(L + ra, cs0, cs1); havecs = true; }
          got1x4(rsv, hoff, hf);
        }
      }
      {
        const f32x4 sq = mfma(hf, hf, zero4);
        const f32x4 sm = mfma(ones, hf, zero4);
        f32x4 al[2], ah[2];
#pragma unroll
        for (int i = 0; i < 2; ++i) { al[i] = mfma(F[i], hf, zero4); ah[i] = mfma(F[6 + i], hf, zero4); }
        const v4u hx = {asu(dpp<0x128>(asf(hf[0]))), asu(dpp<0x128>(asf(hf[1]))), asu(dpp<0x128>(asf(hf[2]))), asu(dpp<0x128>(asf(hf[3])))};
        SB();
#pragma unroll
        for (int i = 0; i < 2; ++i) { al[i] = mfma(F[4 + i], hx, al[i]); ah[i] = mfma(F[2 + i], hx, ah[i]); }
        *(LAS f32x2*)(L + sw) = f32x2{sm[0], asf(pick(m2, pick(m1, asu(sq[3]), asu(sq[2])), pick(m1, asu(sq[1]), asu(sq[0]))))};
#pragma unroll
        for (int i = 0; i < 2; ++i)
          *(LAS v4u*)(L + wa + 32768 + i * 1024) = (__builtin_bit_cast(v4u, al[i]) & mlo) | (__builtin_bit_cast(v4u, ah[i]) & ~mlo);
      }
      if (fin && !havecs) {
#pragma nounroll
        for (unsigned it = 0; it < SPIN_L && !all16(L, L_FL + 64); ++it) __builtin_amdgcn_s_sleep(1);
        CBAR();
        colsum(L + ra, cs0, cs1);
      }
      PIN(cs0); PIN(cs1);
      BAR();
      if (fin) {
        LAS u8* p = L + ra + 32768;
        const f32x4 sa4 = ld4(L + sr), sb4 = ld4(L + sr + 16);
        f32x4 tt[8];
#pragma unroll
        for (int i = 0; i < 8; ++i) tt[i] = ld4(p + i * 2048);
        SB();
        const f32x4 s4 = sa4 + sb4;
        float st = t16(s4[0] + s4[2], s4[1] + s4[3]);
        st += dpp<0x128>(st);
        st = t32(st, st) * (1.f / 1024.f);
        auto sx = __builtin_amdgcn_permlane16_swap(asu(st), asu(st), false, false);
        const float mean = asf(sx[0]);
        const float var = fmaxf(fmaf(-mean, mean, asf(sx[1])), 0.f);
        const f32x4 t = ((tt[0] + tt[1]) + (tt[2] + tt[3])) + ((tt[4] + tt[5]) + (tt[6] + tt[7]));
        const float g0 = t32(t[0], t[2]), g1 = t32(t[1], t[3]);
        const float mb = rb(mean), inv = rcp(rb(__builtin_amdgcn_sqrtf(rb(rb(var) + 1e-5f))));
        const v2u gp = join32(nz(pk(fmaf(-mb, cs0, g0) * inv, fmaf(-mb, cs1, g1) * inv)));
        asm volatile("s_mov_b32 exec_hi, 0\n\tbuffer_store_dwordx2 %0, %1, %2, 0 offen sc1\n\ts_mov_b64 exec, -1" :: "v"(gp), "v"(goff), "s"(rsv) : "memory");
      }
    }
  }
}

extern "C" void kernel_launch(void* const* d_in, const int* in_sizes, int n_in, void* d_out, int out_size, void* d_ws, size_t ws_size,
                              hipStream_t stream) {
  (void)in_sizes; (void)n_in; (void)out_size; (void)ws_size;
  (void)hipFuncSetAttribute((const void*)fused_decode_kernel, hipFuncAttributeMaxDynamicSharedMemorySize, L_END);
  fused_decode_kernel<<<256, 1024, L_END, stream>>>((const u8*)d_in[3], (const u8*)d_in[0], (u8*)d_ws, (const u8*)d_in[1],
                                                    (const u8*)d_in[2], (const u8*)d_in[4], (const u8*)d_in[6], (const u8*)d_in[7],
                                                    (const u8*)d_in[8], (u8*)d_out);
}
```
